```python
import torch

def _softmax(x, axis=-1):
    return torch.softmax(x, dim=axis)

def _gelu(x):
    return torch.nn.functional.gelu(x, approximate='tanh')

def _silu(x):
    return torch.nn.functional.silu(x)

def _sigmoid(x):
    return torch.sigmoid(x)

def _layernorm(x, axis=-1, eps=1e-05):
    m = x.mean(dim=axis, keepdim=True)
    v = x.var(dim=axis, keepdim=True, unbiased=False)
    return (x - m) / torch.sqrt(v + eps)

def _scatter_add(tgt, idx, src, axis=0):
    return tgt.clone().index_add_(axis, idx, src)

def _segment_sum(src, seg, n):
    out = torch.zeros((n,) + src.shape[1:], dtype=src.dtype)
    return out.index_add_(0, seg, src)

def _pad(x, pw):
    flat = tuple((v for pair in reversed(pw) for v in pair))
    return torch.nn.functional.pad(x, flat)
kernel_name = 'parallel_sum__memory_aug_recurrent_mem_tr__dense_activation_mish__prompt'

def setup_inputs():

    _w = lambda a: (lambda t: t.to(torch.bfloat16) if t.is_floating_point() else t)(torch.as_tensor(a))

    def _A_setup():
        B, S, Dm = (1, 2048, 1024)
        H, Dh = (32, 128)
        M, seg = (8, 32)
        seg = min(seg, S)
        S = S // seg * seg
        return {'x': _w((lambda *s: torch.randn(*s, dtype=torch.bfloat16) * 0.05)(B, S, Dm)), 'mem0': _w((lambda *s: torch.randn(*s, dtype=torch.bfloat16) * 0.2)(B, M, Dm)), 'Wq': _w((lambda *s: torch.randn(*s, dtype=torch.bfloat16) * 1.6)(Dm, H, Dh) * Dm ** (-0.5)), 'Wk': _w((lambda *s: torch.randn(*s, dtype=torch.bfloat16) * 1.6)(Dm, H, Dh) * Dm ** (-0.5)), 'Wv': _w((lambda *s: torch.randn(*s, dtype=torch.bfloat16) * 0.2)(Dm, H, Dh) * Dm ** (-0.5)), 'Wo': _w((lambda *s: torch.randn(*s, dtype=torch.bfloat16) * 0.2)(H, Dh, Dm) * (H * Dh) ** (-0.5)), 'seg_len': seg}
    _A_in = _A_setup()

    def _B_setup():
        B, S, D, F = (1, 2048, 1024, 3584)
        return {'x': _w((lambda *s: torch.randn(*s, dtype=torch.bfloat16) * 0.05)(B, S, D)), 'w1': _w((lambda *s: torch.randn(*s, dtype=torch.bfloat16) * 0.4)(D, F) * D ** (-0.5)), 'b1': _w((lambda *s: torch.randn(*s, dtype=torch.bfloat16) * 0.1)(F)), 'w2': _w((lambda *s: torch.randn(*s, dtype=torch.bfloat16) * 0.1)(F, D) * F ** (-0.5))}
    _B_in = _B_setup()
    return {'A_x': _A_in['x'], 'A_mem0': _A_in['mem0'], 'A_Wq': _A_in['Wq'], 'A_Wk': _A_in['Wk'], 'A_Wv': _A_in['Wv'], 'A_Wo': _A_in['Wo'], 'A_seg_len': _A_in['seg_len'], 'B_x': _B_in['x'], 'B_w1': _B_in['w1'], 'B_b1': _B_in['b1'], 'B_w2': _B_in['w2']}

def reference(A_x, A_mem0, A_Wq, A_Wk, A_Wv, A_Wo, A_seg_len, B_x, B_w1, B_b1, B_w2):

    def _A_ref(x, mem0, Wq, Wk, Wv, Wo, seg_len):
        B, S, Dm = x.shape
        H, Dh = (Wq.shape[1], Wq.shape[2])
        M = mem0.shape[1]
        scale = Dh ** (-0.5)
        n_seg = S // seg_len
        L = M + seg_len
        idx = torch.arange(L)
        mask = (idx[:, None] < M) | (idx[:, None] >= idx[None, :])
        mem = mem0
        outs = []
        for s in range(n_seg):
            xs = x[:, s * seg_len:(s + 1) * seg_len, :].clone()
            h = torch.concatenate([mem, xs], axis=1)
            q = torch.einsum('btd,dhe->bhte', h, Wq)
            k = torch.einsum('btd,dhe->bhte', h, Wk)
            v = torch.einsum('btd,dhe->bhte', h, Wv)
            scores = torch.einsum('bhqe,bhke->bhqk', q, k) * scale
            scores = torch.where(mask[None, None], scores, -1000000000.0)
            p = _softmax(scores, axis=-1)
            o = torch.einsum('bhqk,bhke->bhqe', p, v)
            o = torch.einsum('bhte,hed->btd', o, Wo)
            mem = o[:, :M, :].clone()
            outs.append(o[:, M:, :])
        return torch.concatenate(outs, axis=1)

    def _B_ref(x, w1, b1, w2):
        h = torch.einsum('bsd,df->bsf', x, w1) + b1
        sp = torch.log1p(torch.exp(-torch.abs(h))) + torch.where(h > 0, h, torch.zeros_like(h))
        act = h * torch.tanh(sp)
        return torch.einsum('bsf,fd->bsd', act, w2)
    x = A_x
    xn = _layernorm(x, axis=-1)
    return x + _A_ref(xn, A_mem0, A_Wq, A_Wk, A_Wv, A_Wo, A_seg_len) + _B_ref(xn, B_w1, B_b1, B_w2)
```

```cpp
#include <hip/hip_runtime.h>
#include <hip/hip_bf16.h>
#include <stdint.h>

typedef unsigned int u32;
typedef unsigned short u16;
typedef u32 u32x4 __attribute__((ext_vector_type(4)));
typedef u32 u32x2 __attribute__((ext_vector_type(2)));
typedef short bf16x8 __attribute__((ext_vector_type(8)));
typedef short s16x4 __attribute__((ext_vector_type(4)));
typedef float f32x4 __attribute__((ext_vector_type(4)));
typedef __bf16 bf2_t __attribute__((ext_vector_type(2)));
typedef float f32x2 __attribute__((ext_vector_type(2)));
#define LDSP __attribute__((address_space(3)))
#define DEV __device__ __forceinline__

constexpr int S_ = 2048, D_ = 1024, HD = 4096, FF = 3584, NSEG = 64;
constexpr u32 SENT = 0xFFFFFFFFu;
constexpr size_t WS_XN = 0;
constexpr size_t WS_Q = 4u << 20;
constexpr size_t WS_K = 20u << 20;
constexpr size_t WS_V = 36u << 20;
constexpr size_t WS_ACT = 52u << 20;
constexpr size_t WS_O = 68u << 20;
constexpr size_t WS_KM = 84u << 20;
constexpr size_t WS_VM = 88u << 20;
constexpr size_t WS_CTL = 92u << 20;
constexpr size_t WS_XCH = 96u << 20;
constexpr size_t WS_Y = 100u << 20;
constexpr size_t WS_P = 116u << 20;
constexpr u32 SLOT = 256 * 1024, X_M = 0, X_O = 32768, X_SC = 98304;
constexpr int C_TICK = 0;
constexpr int C_QUEUE = 1408  , C_F1 = 512, C_Q = C_F1 + 8 * 32, C_Y = C_Q + 128, C_A = C_Y + 32  , C_P = C_A + 128  , C_KM = 2048;
constexpr int C_OVF = 8 * 32, C_TOTAL = 9 * 32, C_NEAR = 10 * 32, C_TMO = 11 * 32;

DEV u32 cvt_pk(float a, float b) { f32x2 v = {a, b}; bf2_t r = __builtin_convertvector(v, bf2_t); return __builtin_bit_cast(u32, r); }
DEV float bf_lo(u32 x) { return __uint_as_float(x << 16); }
DEV float bf_hi(u32 x) { return __uint_as_float(x & 0xFFFF0000u); }
DEV u32 nosent(u32 x) { return x == SENT ? 0xFFFEFFFFu : x; }
DEV float ror8(float x) { return __int_as_float(__builtin_amdgcn_update_dpp(0, __float_as_int(x), 0x128, 0xF, 0xF, true)); }
DEV u32 ror8u(u32 x) { return (u32)__builtin_amdgcn_update_dpp(0, (int)x, 0x128, 0xF, 0xF, true); }
DEV float xor16(float x) { u32 a = __float_as_uint(x); auto r = __builtin_amdgcn_permlane16_swap(a, a, false, false); return __uint_as_float((threadIdx.x & 16) ? r[0] : r[1]); }
DEV float xor32(float x) { u32 a = __float_as_uint(x); auto r = __builtin_amdgcn_permlane32_swap(a, a, false, false); return __uint_as_float((threadIdx.x & 32) ? r[0] : r[1]); }
DEV f32x4 mfma16(bf16x8 a, bf16x8 b, f32x4 c) { return __builtin_amdgcn_mfma_f32_16x16x32_bf16(a, b, c, 0, 0, 0); }
DEV bf16x8 asfrag(u32x4 v) { return __builtin_bit_cast(bf16x8, v); }
DEV s16x4 trread(const char* p) { return __builtin_amdgcn_ds_read_tr16_b64_v4i16((LDSP s16x4*)p); }
DEV bf16x8 cat4(s16x4 a, s16x4 b) { return __builtin_shufflevector(a, b, 0, 1, 2, 3, 4, 5, 6, 7); }
DEV bool valid4(u32x4 v) { return (v.x != SENT) & (v.y != SENT) & (v.z != SENT) & (v.w != SENT); }
DEV u32x4 ld_sc1(__amdgpu_buffer_rsrc_t rs, u32 off) { return __builtin_amdgcn_raw_buffer_load_b128(rs, off, 0, 16); }
DEV void st_sc1(__amdgpu_buffer_rsrc_t rs, u32 off, u32x4 v) { __builtin_amdgcn_raw_buffer_store_b128(v, rs, off, 0, 16); }
DEV u32 ctl_ld(u32* p) { return __hip_atomic_load(p, __ATOMIC_RELAXED, __HIP_MEMORY_SCOPE_AGENT); }
DEV void ctl_st(u32* p, u32 v) { __hip_atomic_store(p, v, __ATOMIC_RELAXED, __HIP_MEMORY_SCOPE_AGENT); }
DEV u32 ctl_add(u32* p, u32 v) { return __hip_atomic_fetch_add(p, v, __ATOMIC_RELAXED, __HIP_MEMORY_SCOPE_AGENT); }
DEV char* xch_base(char* ws) { return (char*)(((size_t)(ws + WS_XCH) + (2u << 20) - 1) & ~(size_t)((2u << 20) - 1)); }

DEV int tid_of(int wv) { int ln; asm volatile("v_mbcnt_lo_u32_b32 %0, -1, 0\n v_mbcnt_hi_u32_b32 %0, -1, %0" : "=v"(ln)); return wv * 64 + ln; }
template <class T> DEV T* uni(T* p) {
  const unsigned long long a = (unsigned long long)p;
  return (T*)(((unsigned long long)(u32)__builtin_amdgcn_readfirstlane((int)(a >> 32)) << 32) | (u32)__builtin_amdgcn_readfirstlane((int)a));
}

struct Params {
  const u16* x; const u16* mem0; const u16* Wq; const u16* Wk; const u16* Wv; const u16* Wo;
  const u16* w1; const u16* b1; const u16* w2; u16* out; char* ws;
};

__global__ void __launch_bounds__(256) k_prep(Params p) {
  const int tid = threadIdx.x, lane = tid & 63, wave = tid >> 6, b = blockIdx.x;
  {
    const int row = b * 4 + wave;
    const u32x4* src = (const u32x4*)(p.x + (size_t)row * D_);
    u32x4 v0 = src[lane], v1 = src[lane + 64];
    float f[16];
    f[0] = bf_lo(v0.x); f[1] = bf_hi(v0.x); f[2] = bf_lo(v0.y); f[3] = bf_hi(v0.y); f[4] = bf_lo(v0.z); f[5] = bf_hi(v0.z); f[6] = bf_lo(v0.w); f[7] = bf_hi(v0.w);
    f[8] = bf_lo(v1.x); f[9] = bf_hi(v1.x); f[10] = bf_lo(v1.y); f[11] = bf_hi(v1.y); f[12] = bf_lo(v1.z); f[13] = bf_hi(v1.z); f[14] = bf_lo(v1.w); f[15] = bf_hi(v1.w);
    float s = 0; for (int i = 0; i < 16; ++i) s += f[i];
    for (int o = 32; o; o >>= 1) s += __shfl_xor(s, o);
    const float mean = s * (1.f / D_);
    float q = 0; for (int i = 0; i < 16; ++i) { f[i] -= mean; q += f[i] * f[i]; }
    for (int o = 32; o; o >>= 1) q += __shfl_xor(q, o);
    const float rs = 1.f / sqrtf(q * (1.f / D_) + 1e-5f);
    u32x4 o0 = {cvt_pk(f[0] * rs, f[1] * rs), cvt_pk(f[2] * rs, f[3] * rs), cvt_pk(f[4] * rs, f[5] * rs), cvt_pk(f[6] * rs, f[7] * rs)};
    u32x4 o1 = {cvt_pk(f[8] * rs, f[9] * rs), cvt_pk(f[10] * rs, f[11] * rs), cvt_pk(f[12] * rs, f[13] * rs), cvt_pk(f[14] * rs, f[15] * rs)};
    u32x4* dst = (u32x4*)(p.ws + WS_XN + (size_t)row * D_ * 2);
    dst[lane] = o0; dst[lane + 64] = o1;
  }
  {
    const u32 off = (u32)(b * 256 + tid) * 16;
    const u32 x = ((off >> 14) << 13) | (off & 8191);
    u32x4 v = {SENT, SENT, SENT, SENT};
    if (x < 32768) {
      const u32 c8 = x >> 8, g = (x >> 7) & 1, r = (x >> 4) & 7;
      if (g == 0) { v = *(const u32x4*)(p.mem0 + r * D_ + c8 * 8); v.x = nosent(v.x); v.y = nosent(v.y); v.z = nosent(v.z); v.w = nosent(v.w); }
      else v = u32x4{0, 0, 0, 0};
    }
    *(u32x4*)(xch_base(p.ws) + off) = v;
  }
  if (b == 0) { u32* ctl = (u32*)(p.ws + WS_CTL); for (int i = tid; i < 4096; i += 256) ctl[i] = 0; }
}

DEV int take_role(u32* ctl, int* s_tmp) {
  if (threadIdx.x == 0) {
    u32 xcc; asm volatile("s_getreg_b32 %0, hwreg(HW_REG_XCC_ID)" : "=s"(xcc)); xcc &= 7;
    const u32 t = ctl_add(ctl + C_TICK + xcc * 32, 1);
    int role;
    if (t < 32) { role = xcc * 32 + t; ctl_add(ctl + C_TOTAL, 1); }
    else {
      const u32 k = ctl_add(ctl + C_OVF, 1); ctl_add(ctl + C_TOTAL, 1);
      int sp = 0; while (ctl_ld(ctl + C_TOTAL) < gridDim.x && ++sp < 2000000) __builtin_amdgcn_s_sleep(2);
      role = -1; u32 n = 0;
      for (int xx = 0; xx < 8; ++xx) { const u32 c = ctl_ld(ctl + C_TICK + xx * 32); if (c < 32) { if (k < n + (32 - c)) { role = xx * 32 + c + (k - n); break; } n += 32 - c; } }
    }
    *s_tmp = role;
  }
  __syncthreads();
  return __builtin_amdgcn_readfirstlane(*s_tmp);
}
DEV float red16_add(float x) { u32 a = __float_as_uint(x); auto r = __builtin_amdgcn_permlane16_swap(a, a, false, false); return __uint_as_float(r[0]) + __uint_as_float(r[1]); }
DEV float red32_add(float x) { u32 a = __float_as_uint(x); auto r = __builtin_amdgcn_permlane32_swap(a, a, false, false); return __uint_as_float(r[0]) + __uint_as_float(r[1]); }
DEV float red16_max(float x) { u32 a = __float_as_uint(x); auto r = __builtin_amdgcn_permlane16_swap(a, a, false, false); return fmaxf(__uint_as_float(r[0]), __uint_as_float(r[1])); }
DEV float red32_max(float x) { u32 a = __float_as_uint(x); auto r = __builtin_amdgcn_permlane32_swap(a, a, false, false); return fmaxf(__uint_as_float(r[0]), __uint_as_float(r[1])); }
DEV f32x4 halfsum4(f32x4 a, f32x4 b) {
  float d0, d1, d2, d3;
  asm volatile("s_nop 7\n s_nop 3\n"
               "v_add_f32_dpp %0, %4, %4 row_ror:8 row_mask:0xf bank_mask:0x3\n v_add_f32_dpp %1, %5, %5 row_ror:8 row_mask:0xf bank_mask:0x3\n"
               "v_add_f32_dpp %2, %6, %6 row_ror:8 row_mask:0xf bank_mask:0x3\n v_add_f32_dpp %3, %7, %7 row_ror:8 row_mask:0xf bank_mask:0x3\n"
               "v_add_f32_dpp %0, %8, %8 row_ror:8 row_mask:0xf bank_mask:0xc\n v_add_f32_dpp %1, %9, %9 row_ror:8 row_mask:0xf bank_mask:0xc\n"
               "v_add_f32_dpp %2, %10, %10 row_ror:8 row_mask:0xf bank_mask:0xc\n v_add_f32_dpp %3, %11, %11 row_ror:8 row_mask:0xf bank_mask:0xc\n s_nop 1"
               : "=&v"(d0), "=&v"(d1), "=&v"(d2), "=&v"(d3)
               : "v"(a[0]), "v"(a[1]), "v"(a[2]), "v"(a[3]), "v"(b[0]), "v"(b[1]), "v"(b[2]), "v"(b[3]));
  return f32x4{d0, d1, d2, d3};
}
DEV u32 nos(u32 x) { return min(x, 0xFFFFFFFEu); }
#define BARL() asm volatile("s_waitcnt lgkmcnt(0)\n s_barrier" ::: "memory")
DEV void nap(int n) { for (int i = 0; i < n; ++i) __builtin_amdgcn_s_sleep(1); }
#define CH_D1 12
#define CH_D3 10
#define CH_D2 8
#define SMW 2
constexpr int SPIN_CAP = 150000;
constexpr int L_RQK = 0;
constexpr int L_RV = 8 * 584 * 4;
constexpr int L_R2 = L_RV + 8 * 296 * 4;
constexpr int L_KT = L_R2 + 8 * 160 * 4;
constexpr int L_VT = L_KT + 2 * 32 * 64;
constexpr int L_VM = L_VT + 2 * 32 * 64;
constexpr int L_F = L_VM + 16 * 64 + 64;
constexpr int L_W2 = L_F + 1024;
constexpr int L_CHAIN_END = L_VM + 16 * 64;

#define POLL(N, V, LDEXPR, NSP) do { \
    if (dead) { _Pragma("unroll") for (int k = 0; k < N; ++k) V[k] = u32x4{0, 0, 0, 0}; } \
    else for (int sp = 0;; ++sp) { \
      asm volatile("" ::: "memory"); \
      _Pragma("unroll") for (int k = 0; k < N; ++k) V[k] = (LDEXPR); \
      u32 mxv = 0; \
      _Pragma("unroll") for (int k = 0; k < N; ++k) mxv = max(max(mxv, max(V[k].x, V[k].y)), max(V[k].z, V[k].w)); \
      if (__all(mxv != SENT)) { NSP += sp; break; } \
      if (sp > SPIN_CAP || ((sp & 255) == 255 && __builtin_amdgcn_readfirstlane(ctl_ld(ctl + C_TMO)))) { ctl_st(ctl + C_TMO, 1); dead = true; break; } \
    } } while (0)
DEV u32x4 ldx(__amdgpu_buffer_rsrc_t rs, u32 voff, u32 soff) { return __builtin_amdgcn_raw_buffer_load_b128(rs, voff, soff, 16); }
DEV void stx(__amdgpu_buffer_rsrc_t rs, u32 voff, u32 soff, u32x4 v) { __builtin_amdgcn_raw_buffer_store_b128(v, rs, voff, soff, 16); }

DEV void chain_role(const Params& p, const int role, char* smem) {
  const int tid = threadIdx.x, w = __builtin_amdgcn_readfirstlane(tid >> 6), l = tid & 63, q = l >> 4, i = l & 15, r = l & 7, g2 = (l >> 3) & 1;
  const int h = role >> 2, j = role & 3, cs = role >> 1, g = role & 1;
  u32* ctl = (u32*)(p.ws + WS_CTL);
  char* xb = xch_base(p.ws);
  if (role == 0 && w == 0) {
    u32 lat[2] = {1u << 30, 1u << 30};
    for (int rep = 0; rep < 24; ++rep) {
      const int c = rep & 1;
      u32* pq = (u32*)(xb + 127 * 16384 + c * 8192 + 8064) + (l & 31);
      const long long t0 = clock64();
      asm volatile("global_store_dword %0, %1, off sc1\n s_waitcnt vmcnt(0)" ::"v"(pq), "v"(SENT) : "memory");
      const u32 dt = (u32)(clock64() - t0);
      if (c) lat[1] = min(lat[1], dt); else lat[0] = min(lat[0], dt);
    }
    if (l == 0) { ctl[C_NEAR + 1] = lat[0]; ctl[C_NEAR + 2] = lat[1]; ctl_st(ctl + C_NEAR, lat[1] < lat[0] ? 2u : 1u); }
  }
  bf16x8 W1[6][4];
  bf16x8 W2t[8];
  {
    char* stg = smem + w * 8192;
    const u16* mats[3] = {p.Wq, p.Wk, p.Wv};
    u32x4 raw[4][8];
#pragma unroll
    for (int m = 0; m < 3; ++m) {
      const u16* src = mats[m] + (size_t)(128 * w) * HD + h * 128 + 32 * j;
#pragma unroll
      for (int it = 0; it < 8; ++it) raw[m][it] = *(const u32x4*)(src + (size_t)(it * 16 + (l >> 2)) * HD + (l & 3) * 8);
    }
    {
      const u16* src = p.Wo + (size_t)(2048 * g + 256 * w) * D_ + 16 * cs;
#pragma unroll
      for (int it = 0; it < 8; ++it) raw[3][it] = *(const u32x4*)(src + (size_t)(it * 32 + (l >> 1)) * D_ + (l & 1) * 8);
    }
    __builtin_amdgcn_sched_barrier(0);
#pragma unroll
    for (int m = 0; m < 3; ++m) {
#pragma unroll
      for (int it = 0; it < 8; ++it) *(u32x4*)(stg + (it * 16 + (l >> 2)) * 64 + (l & 3) * 16) = raw[m][it];
      asm volatile("s_waitcnt lgkmcnt(0)" ::: "memory");
#pragma unroll
      for (int t = 0; t < 2; ++t)
#pragma unroll
        for (int ks = 0; ks < 4; ++ks) {
          const char* a = stg + (32 * ks + 8 * q + (i >> 2)) * 64 + t * 32 + (i & 3) * 8;
          W1[m * 2 + t][ks] = cat4(trread(a), trread(a + 4 * 64));
        }
      asm volatile("s_waitcnt lgkmcnt(0)" ::: "memory");
    }
#pragma unroll
    for (int it = 0; it < 8; ++it) *(u32x4*)(stg + (it * 32 + (l >> 1)) * 32 + (l & 1) * 16) = raw[3][it];
    asm volatile("s_waitcnt lgkmcnt(0)" ::: "memory");
#pragma unroll
    for (int ks = 0; ks < 8; ++ks) { const char* a = stg + (32 * ks + 8 * q + (i >> 2)) * 32 + (i & 3) * 8; W2t[ks] = cat4(trread(a), trread(a + 4 * 32)); }
    asm volatile("s_waitcnt lgkmcnt(0)" ::: "memory");
  }
  __syncthreads();
#pragma unroll
  for (int ks = 0; ks < 8; ++ks) *(bf16x8*)(smem + L_W2 + (w * 8 + ks) * 1024 + l * 16) = W2t[ks];
  const __amdgpu_buffer_rsrc_t rkv = __builtin_amdgcn_make_buffer_rsrc((void*)((const u16*)(p.ws + WS_K) + h * 128 + 32 * j), 0, 0x7FFFFFFF, 0x00020000);
  const u32 vkv = (u32)((l >> 2) * HD + (l & 3) * 8) * 2u;
  auto prefetch = [&](int s) {
    char* d = smem + L_KT + (s & 1) * 2048;
    const u32 so = (u32)s * 32 * HD * 2;
    __builtin_amdgcn_raw_ptr_buffer_load_lds(rkv, (LDSP void*)d, 16, vkv, so, 0, 0);
    __builtin_amdgcn_raw_ptr_buffer_load_lds(rkv, (LDSP void*)(d + 1024), 16, vkv, so + 16 * HD * 2, 0, 0);
    __builtin_amdgcn_raw_ptr_buffer_load_lds(rkv, (LDSP void*)(d + 4096), 16, vkv, so + (u32)(WS_V - WS_K), 0, 0);
    __builtin_amdgcn_raw_ptr_buffer_load_lds(rkv, (LDSP void*)(d + 4096 + 1024), 16, vkv, so + (u32)(WS_V - WS_K) + 16 * HD * 2, 0, 0);
  };
  if (w == 7) { prefetch(0); asm volatile("s_waitcnt vmcnt(0)" ::: "memory"); }
  if (w == 0 && l < 32) *(u32x4*)(smem + L_VM + 512 + l * 16) = u32x4{0, 0, 0, 0};
  if (tid == 0) { int sp = 0; u32 c; while ((c = ctl_ld(ctl + C_NEAR)) == 0 && ++sp < 4000000) __builtin_amdgcn_s_sleep(1); *(volatile u32*)(smem + L_CHAIN_END) = c; }
  __syncthreads();
  const u32 near = (__builtin_amdgcn_readfirstlane(*(volatile u32*)(smem + L_CHAIN_END)) == 2) ? 8192u : 0u;
  const __amdgpu_buffer_rsrc_t rs = __builtin_amdgcn_make_buffer_rsrc((void*)(xb + near), 0, 0x7FFFFFFF, 0x00020000);
  auto PH = [](u32 x) -> u32 { return ((x >> 13) << 14) | (x & 8191); };
  constexpr u32 X_S0 = X_SC + 131072;
  const u32 oM = PH(X_M + 4 * w * 1024 + l * 16), oO = PH(X_O + g * 32768 + (32 * w + 4 * g2 + q) * 128 + r * 16);
  const u32 oS = PH(X_SC + h * 4096 + g2 * 512 + q * 128 + r * 16);
  const u32 oS0 = PH(X_S0 + h * 1024 + ((q >> 1) * 2 + g2) * 256 + (q & 1) * 128 + r * 16);
  const u32 pS0 = PH(X_S0 + h * 1024 + j * 256 + (q & 1) * 128 + r * 16);
  const u32 pO = PH(X_O + role * 512 + q * 128 + r * 16);
  const u32 pM = PH(X_M + (2 * cs + ((l >> 2) & 1)) * 256 + g * 128 + (l >> 3) * 16);
  const u32 jo = j * 1024;
  float* RQK = (float*)(smem + L_RQK); float* RV = (float*)(smem + L_RV); float* R2 = (float*)(smem + L_R2);
  const __amdgpu_buffer_rsrc_t rkm = __builtin_amdgcn_make_buffer_rsrc((void*)((u16*)(p.ws + WS_KM) + h * 128 + 32 * j), 0, 0x7FFFFFFF, 0x00020000);
  bool dead = false; int ns1 = 0, ns2 = 0, ns3 = 0;
#define TS(k)
  const u32x4 SV = {SENT, SENT, SENT, SENT};

  auto reset = [&](int s) {
    const u32 so = (u32)((s - 2) & 3) * (2 * SLOT);
    stx(rs, oS, so + jo, SV); if (l < 32 && g2 == 0) stx(rs, pS0, so, SV);
    if (g2 == 0) stx(rs, pO, so, SV);
    if ((l & 3) == 0) stx(rs, pM, so, SV);
  };
#define RESET() do { if (w == 6 && s >= 2) reset(s); } while (0)
#define PFPOS 4
#define RSPOS 0
  for (int s = 0; s < NSEG; ++s) {
    const u32 sb = (u32)(s & 3) * (2 * SLOT);
    u32x4 mv[4];
    if (s) nap(w == 1 ? CH_D1 - 4 : CH_D1);
    TS(0);
    f32x4 acc[6];
    const f32x4 z4 = {0, 0, 0, 0};
    {
#pragma unroll
      for (int k = 0; k < 4; ++k) mv[k] = ldx(rs, oM + k * 1024, sb);
      if (PFPOS == 4 && w == 7 && s + 1 < NSEG) prefetch(s + 1);
#pragma unroll
      for (int ks = 0; ks < 4; ++ks)
#pragma unroll
        for (int t = 0; t < 4; ++t) acc[t] = mfma16(W1[t][ks], asfrag(mv[ks]), ks ? acc[t] : z4);
      u32 mxv = 0;
#pragma unroll
      for (int k = 0; k < 4; ++k) mxv = max(max(mxv, max(mv[k].x, mv[k].y)), max(mv[k].z, mv[k].w));
      if (!__all(mxv != SENT)) {
        ++ns1;
        POLL(4, mv, ldx(rs, oM + k * 1024, sb), ns1);
#pragma unroll
        for (int ks = 0; ks < 4; ++ks)
#pragma unroll
          for (int t = 0; t < 4; ++t) acc[t] = mfma16(W1[t][ks], asfrag(mv[ks]), ks ? acc[t] : z4);
      }
    }
    TS(1);
    {
      f32x4 a, b;
      a = halfsum4(acc[0], acc[2]); b = halfsum4(acc[1], acc[3]);
      float* d = RQK + w * 584 + g2 * 288 + r * 36 + 4 * q;
      *(f32x4*)d = a; *(f32x4*)(d + 16) = b;
    }
    BARL();
    TS(2);
    if (PFPOS == 0 && w == 7 && s + 1 < NSEG) prefetch(s + 1);
    if (RSPOS == 0) RESET();
    {
      const float* b = RQK + (l >> 5) * 288 + w * 36 + (l & 31);
      float v[8];
#pragma unroll
      for (int ww = 0; ww < 8; ++ww) v[ww] = b[ww * 584];
      __builtin_amdgcn_sched_barrier(0);
      const float t = ((v[0] + v[1]) + (v[2] + v[3])) + ((v[4] + v[5]) + (v[6] + v[7]));
      const float o = __int_as_float(__builtin_amdgcn_update_dpp(0, __float_as_int(t), 0xB1, 0xF, 0xF, true));
      if ((l & 1) == 0) *(u32*)(smem + L_F + (l >> 5) * 512 + w * 64 + (l & 31) * 2) = cvt_pk(t, o);
    }
    BARL();
    if (PFPOS == 3 && w == 7 && s + 1 < NSEG) prefetch(s + 1);
    if (RSPOS == 3) RESET();
    if (w == 0) {
      const u32x4 qf = *(const u32x4*)(smem + L_F + r * 64 + q * 16);
      const u32x4 kfrag = *(const u32x4*)(smem + L_F + 512 + r * 64 + q * 16);
      const char* kt = smem + L_KT + (s & 1) * 2048 + i * 64 + q * 16;
      const bf16x8 kt0 = *(const bf16x8*)kt, kt1 = *(const bf16x8*)(kt + 1024);
      const f32x4 z = {0, 0, 0, 0};
      const f32x4 c1 = mfma16(kt0, asfrag(qf), z);
      const f32x4 c2 = mfma16(kt1, asfrag(qf), z);
      const f32x4 c0 = mfma16(asfrag(kfrag), asfrag(qf), z);
      if (s < NSEG - 1) {
        const u32x4 v12 = {nos(__float_as_uint(g2 ? c2[0] : c1[0])), nos(__float_as_uint(g2 ? c2[1] : c1[1])), nos(__float_as_uint(g2 ? c2[2] : c1[2])), nos(__float_as_uint(g2 ? c2[3] : c1[3]))};
        stx(rs, oS, sb + jo, v12);
        const u32x4 v0 = {nos(__float_as_uint(c0[0])), nos(__float_as_uint(c0[1])), nos(__float_as_uint(c0[2])), nos(__float_as_uint(c0[3]))};
        if (l < 32 && g2 == 0) stx(rs, pS0, sb, v0);
      }
      if (g2 == 0) __builtin_amdgcn_raw_buffer_store_b128(kfrag, rkm, (r * HD + 8 * q) * 2, s * 8 * HD * 2, 16);
    }
    TS(3);
#pragma unroll
    for (int ks = 0; ks < 4; ++ks) { acc[4] = mfma16(W1[4][ks], asfrag(mv[ks]), ks ? acc[4] : z4); acc[5] = mfma16(W1[5][ks], asfrag(mv[ks]), ks ? acc[5] : z4); }
    {
      f32x4 a;
      a = halfsum4(acc[4], acc[5]);
      *(f32x4*)(RV + w * 296 + r * 36 + 16 * g2 + 4 * q) = a;
    }
    u32x4 sv[5];
    if (w == SMW && s < NSEG - 1) {
      nap(CH_D2);
#pragma unroll
      for (int k = 0; k < 5; ++k) sv[k] = k < 4 ? ldx(rs, oS + k * 1024, sb) : ldx(rs, oS0, sb);
    }
    BARL();
    if (PFPOS == 2 && w == 7 && s + 1 < NSEG) prefetch(s + 1);
    if (RSPOS == 2) RESET();
    TS(4);
    if (w == SMW) {
      f32x4 rd[4][2];
#pragma unroll
      for (int ww = 0; ww < 4; ++ww) { const float* b = RV + (4 * g2 + ww) * 296 + r * 36 + 8 * q; rd[ww][0] = *(const f32x4*)b; rd[ww][1] = *(const f32x4*)(b + 4); }
      __builtin_amdgcn_sched_barrier(0);
      f32x4 s0 = (rd[0][0] + rd[1][0]) + (rd[2][0] + rd[3][0]), s1 = (rd[0][1] + rd[1][1]) + (rd[2][1] + rd[3][1]);
#pragma unroll
      for (int e = 0; e < 4; ++e) { s0[e] += ror8(s0[e]); s1[e] += ror8(s1[e]); }
      const u32x4 vf = {cvt_pk(s0[0], s0[1]), cvt_pk(s0[2], s0[3]), cvt_pk(s1[0], s1[1]), cvt_pk(s1[2], s1[3])};
      if (g2 == 0) *(u32x4*)(smem + L_VM + r * 64 + q * 16) = vf;
      if (s < NSEG - 1) {
        const char* vt = smem + L_VT + (s & 1) * 2048 + (4 * q + (i >> 2)) * 64 + (i & 3) * 16;
        const char* vm = smem + L_VM + (4 * q + (i >> 2)) * 64 + (i & 3) * 16;
        const bf16x8 vt0 = cat4(trread(vt), trread(vt + 1024)), vt1 = cat4(trread(vt + 8), trread(vt + 1024 + 8));
        const s16x4 zz = {0, 0, 0, 0};
        const bf16x8 vm0 = cat4(trread(vm), zz), vm1 = cat4(trread(vm + 8), zz);
        TS(5);
        {
          u32 mxv = 0;
#pragma unroll
          for (int k = 0; k < 5; ++k) mxv = max(max(mxv, max(sv[k].x, sv[k].y)), max(sv[k].z, sv[k].w));
          if (!__all(mxv != SENT)) { ++ns2; POLL(5, sv, (k < 4 ? ldx(rs, oS + k * 1024, sb) : ldx(rs, oS0, sb)), ns2); }
        }
        TS(6);
        const float sc = 0.08838834764831845f * 1.4426950408889634f;
        float st[4], sm[4];
        st[0] = (__uint_as_float(sv[0].x) + __uint_as_float(sv[1].x)) + (__uint_as_float(sv[2].x) + __uint_as_float(sv[3].x));
        st[1] = (__uint_as_float(sv[0].y) + __uint_as_float(sv[1].y)) + (__uint_as_float(sv[2].y) + __uint_as_float(sv[3].y));
        st[2] = (__uint_as_float(sv[0].z) + __uint_as_float(sv[1].z)) + (__uint_as_float(sv[2].z) + __uint_as_float(sv[3].z));
        st[3] = (__uint_as_float(sv[0].w) + __uint_as_float(sv[1].w)) + (__uint_as_float(sv[2].w) + __uint_as_float(sv[3].w));
        sm[0] = __uint_as_float(sv[4].x); sm[1] = __uint_as_float(sv[4].y); sm[2] = __uint_as_float(sv[4].z); sm[3] = __uint_as_float(sv[4].w);
#pragma unroll
        for (int e = 0; e < 4; ++e) sm[e] = red32_add(sm[e] + ror8(sm[e]));
        float mx = fmaxf(fmaxf(fmaxf(st[0], st[1]), fmaxf(st[2], st[3])), fmaxf(fmaxf(sm[0], sm[1]), fmaxf(sm[2], sm[3])));
        mx = fmaxf(mx, ror8(mx)); mx = red32_max(red16_max(mx));
        const float nm = -mx * sc;
#pragma unroll
        for (int e = 0; e < 4; ++e) { st[e] = __builtin_amdgcn_exp2f(fmaf(st[e], sc, nm)); sm[e] = __builtin_amdgcn_exp2f(fmaf(sm[e], sc, nm)); }
        const u32 p0 = cvt_pk(st[0], st[1]), p1 = cvt_pk(st[2], st[3]);
        const u32 m0 = cvt_pk(sm[0], sm[1]), m1 = cvt_pk(sm[2], sm[3]);
        const u32x4 pt = {p0, p1, ror8u(p0), ror8u(p1)};
        const u32x4 pm = {q < 2 ? m0 : 0u, q < 2 ? m1 : 0u, 0u, 0u};
        const f32x4 z = {0, 0, 0, 0};
        const f32x4 o0 = mfma16(vm0, asfrag(pm), mfma16(vt0, asfrag(pt), z)), o1 = mfma16(vm1, asfrag(pm), mfma16(vt1, asfrag(pt), z));
        float ts = (bf_lo(p0) + bf_hi(p0)) + (bf_lo(p1) + bf_hi(p1)), ms = (bf_lo(m0) + bf_hi(m0)) + (bf_lo(m1) + bf_hi(m1));
        ts += ror8(ts); ts = red32_add(red16_add(ts)); ms = red16_add(ms);
        const float inv = __builtin_amdgcn_rcpf(ts + ms);
        const u32x4 ov = {nos(cvt_pk(o0[0] * inv, o0[1] * inv)), nos(cvt_pk(o0[2] * inv, o0[3] * inv)), nos(cvt_pk(o1[0] * inv, o1[1] * inv)), nos(cvt_pk(o1[2] * inv, o1[3] * inv))};
        if (g2 == 0) stx(rs, pO, sb, ov);
      }
      if (g2 == 0) __builtin_amdgcn_raw_buffer_store_b128(vf, rkm, (4u << 20) + (r * HD + 8 * q) * 2, s * 8 * HD * 2, 16);
    }
    TS(7);
    if (s == NSEG - 1) {
      asm volatile("s_waitcnt vmcnt(0)" ::: "memory");
      BARL();
      if (tid == 0) ctl_add(ctl + C_KM + s * 32 + h, 1);
      break;
    }
    BARL();
    if (PFPOS == 1 && w == 7) prefetch(s + 1);
    if (RSPOS == 1) RESET();
    u32x4 ov[4];
    bf16x8 W2[8];
#pragma unroll
    for (int ks = 0; ks < 8; ++ks) W2[ks] = *(const bf16x8*)(smem + L_W2 + (w * 8 + ks) * 1024 + l * 16);
    nap(CH_D3); TS(8);
    f32x4 a0, a1;
    {
#pragma unroll
      for (int k = 0; k < 4; ++k) ov[k] = ldx(rs, oO + k * 1024, sb);
#pragma unroll
      for (int k = 0; k < 4; ++k) { a0 = mfma16(W2[2 * k], asfrag(ov[k]), k ? a0 : z4); a1 = mfma16(W2[2 * k + 1], asfrag(ov[k]), k ? a1 : z4); }
      u32 mxv = 0;
#pragma unroll
      for (int k = 0; k < 4; ++k) mxv = max(max(mxv, max(ov[k].x, ov[k].y)), max(ov[k].z, ov[k].w));
      if (!__all(mxv != SENT)) {
        ++ns3;
        POLL(4, ov, ldx(rs, oO + k * 1024, sb), ns3);
#pragma unroll
        for (int k = 0; k < 4; ++k) { a0 = mfma16(W2[2 * k], asfrag(ov[k]), k ? a0 : z4); a1 = mfma16(W2[2 * k + 1], asfrag(ov[k]), k ? a1 : z4); }
      }
    }
    TS(9);
    {
      f32x4 a;
#pragma unroll
      for (int e = 0; e < 4; ++e) a[e] = a0[e] + ror8(a1[e]);
      if (g2 == 0) *(f32x4*)(R2 + w * 160 + r * 20 + 4 * q) = a;
    }
    asm volatile("s_waitcnt vmcnt(0)" ::: "memory");
    BARL();
    if (tid == 192) ctl_add(ctl + C_KM + s * 32 + h, 1);
    TS(10);
    if (w == 1) {
      const int n2 = l & 7, rr = l >> 3;
      f32x2 rr8[8];
#pragma unroll
      for (int ww = 0; ww < 8; ++ww) rr8[ww] = *(const f32x2*)(R2 + ww * 160 + rr * 20 + 2 * n2);
      __builtin_amdgcn_sched_barrier(0);
      const f32x2 sum = ((rr8[0] + rr8[1]) + (rr8[2] + rr8[3])) + ((rr8[4] + rr8[5]) + (rr8[6] + rr8[7]));
      const u32 v = nos(cvt_pk(sum[0], sum[1]));
      const u32x4 o = {(u32)__builtin_amdgcn_update_dpp(0, (int)v, 0x00, 0xF, 0xF, true), (u32)__builtin_amdgcn_update_dpp(0, (int)v, 0x55, 0xF, 0xF, true),
                       (u32)__builtin_amdgcn_update_dpp(0, (int)v, 0xAA, 0xF, 0xF, true), (u32)__builtin_amdgcn_update_dpp(0, (int)v, 0xFF, 0xF, 0xF, true)};
      if ((l & 3) == 0) stx(rs, pM, (u32)((s + 1) & 3) * (2 * SLOT), o);
    }
  }
}
constexpr int G_HT = 128 * 64;
DEV int lds_byte(int r, int c) { int st = (r >> 4) * 2 + (c >> 5), rr = r & 15, cc = c & 31, ob = rr * 64 + cc * 2; return st * 1024 + (ob ^ (((ob >> 9) & 1) << 5)); }
DEV void stage_rc(int b, int& R, int& C) { int st = b / 1024, sb = b % 1024, swz = sb ^ (((sb >> 9) & 1) << 5); R = (st >> 1) * 16 + swz / 64; C = (st & 1) * 32 + (swz % 64) / 2; }
DEV int swzb(int row) { return ((row & 3) << 2) | ((row >> 2) & 3); }
DEV float mish(float h) {
  const float u = __expf(-fabsf(h));
  const float n = h >= 0.f ? 1.f + 2.f * u : u * (u + 2.f);
  const float d = h >= 0.f ? n + 2.f * u * u : n + 2.f;
  return h * n * __builtin_amdgcn_rcpf(d);
}
DEV void gemm256(const int wv, const int MODE, const u16* __restrict__ A, const int lda, const u16* __restrict__ B, const int ldb, const int K, void* C, const int ldc, const u16* bias, char* smem) {
  u16* shm = (u16*)smem;
#define SA(b, h) (shm + ((b) * 2 + (h)) * G_HT)
#define SB(b, h) (shm + (4 + (b) * 2 + (h)) * G_HT)
#define STA_X(T, VO, P, br, kt) do { const int _s = ((br) * lda + (kt) * 64) * 2; \
    __builtin_amdgcn_raw_ptr_buffer_load_lds(rsa, (LDSP void*)((char*)(P) + (T) * 16), 16, (VO), _s, 0, 0); \
    __builtin_amdgcn_raw_ptr_buffer_load_lds(rsa, (LDSP void*)((char*)(P) + (T) * 16 + 8192), 16, (VO), _s + 128 * lda, 0, 0); } while (0)
#define STB_X(T, VO, P, bc, kt) do { const int _s = ((kt) * 64 * ldb + (bc)) * 2; \
    __builtin_amdgcn_raw_ptr_buffer_load_lds(rsb, (LDSP void*)((char*)(P) + (T) * 16), 16, (VO), _s, 0, 0); \
    __builtin_amdgcn_raw_ptr_buffer_load_lds(rsb, (LDSP void*)((char*)(P) + (T) * 16 + 8192), 16, (VO), _s + 64 * ldb, 0, 0); } while (0)
#define STA(P, br, kt) STA_X(t0, voa, P, br, kt)
#define STB(P, bc, kt) STB_X(t0, vob, P, bc, kt)
#define LDA(dst, b, h) for (int m = 0; m < 4; ++m) for (int k = 0; k < 2; ++k) dst[m][k] = *reinterpret_cast<const bf16x8*>((char*)SA(b, h) + lds_byte(wr * 64 + m * 16 + fr, k * 32 + fq * 8))
#define LDB(dst, b, h) for (int n = 0; n < 2; ++n) for (int k = 0; k < 2; ++k) { const char* _p = (const char*)SB(b, h) + k * 8192; \
      dst[n][k] = cat4(trread(_p + bo[n][0]), trread(_p + bo[n][1])); }
#define MMA(ai, bj, At, Bt) do { __builtin_amdgcn_s_setprio(1); \
    for (int m = 0; m < 4; ++m) for (int n = 0; n < 2; ++n) for (int k = 0; k < 2; ++k) acc[ai][bj][m][n] = mfma16(Bt[n][k], At[m][k], acc[ai][bj][m][n]); \
    __builtin_amdgcn_s_setprio(0); } while (0)
#define WAIT_V(n) asm volatile("s_waitcnt vmcnt(" #n ")" ::: "memory")
#define WAIT_L(n) asm volatile("s_waitcnt lgkmcnt(" #n ")" ::: "memory")
#define BAR __builtin_amdgcn_s_barrier()
#define SCHED __builtin_amdgcn_sched_barrier(0)
  const int t0 = tid_of(wv);
  int wid = t0 >> 6, lane = t0 & 63, wr = wid >> 2, wc = wid & 3, fr = lane & 15, fq = lane >> 4;
  f32x4 acc[2][2][4][2] = {};
  bf16x8 At[4][2], B0[2][2], B1[2][2];
  int bo[2][2];
  for (int n = 0; n < 2; ++n) for (int t = 0; t < 2; ++t) { const int row = 8 * fq + 4 * t + (fr >> 2), ch = 4 * wc + (fr & 3); bo[n][t] = 256 * row + 16 * (ch ^ swzb(row)) + 8 * n; }
  const int nt = K / 64;
  const __amdgpu_buffer_rsrc_t rsa = __builtin_amdgcn_make_buffer_rsrc((void*)uni(A), 0, 0x7FFFFFFF, 0x00020000), rsb = __builtin_amdgcn_make_buffer_rsrc((void*)uni(B), 0, 0x7FFFFFFF, 0x00020000);
  u32 voa, vob;
  { int _r, _c; stage_rc(t0 * 16, _r, _c); voa = (u32)(_r * lda + _c) * 2u; const int _b = t0 * 16, _rb = _b >> 8; vob = (u32)(_rb * ldb + ((((_b >> 4) & 15) ^ swzb(_rb)) * 8)) * 2u; }
  STB(SB(0, 0), 0, 0); STA(SA(0, 0), 0, 0); STB(SB(0, 1), 128, 0); STA(SA(0, 1), 128, 0);
  if (wr == 1) BAR;
  WAIT_V(4); BAR;
  STB(SB(1, 0), 0, 1); STA(SA(1, 0), 0, 1); STB(SB(1, 1), 128, 1);
  WAIT_V(6); BAR;
  for (int t = 0; t < nt - 2; t += 2) {
    LDB(B0, 0, 0); SCHED; LDA(At, 0, 0); STA(SA(1, 1), 128, t + 1);
    WAIT_L(8); BAR; WAIT_L(0); MMA(0, 0, At, B0); BAR; SCHED;
    LDB(B1, 0, 1); STB(SB(0, 0), 0, t + 2);
    BAR; WAIT_L(0); MMA(0, 1, At, B1); BAR;
    LDA(At, 0, 1); STA(SA(0, 0), 0, t + 2);
    BAR; WAIT_L(0); MMA(1, 0, At, B0); BAR; SCHED;
    STB(SB(0, 1), 128, t + 2);
    WAIT_V(6); BAR; MMA(1, 1, At, B1); BAR;
    LDB(B0, 1, 0); SCHED; LDA(At, 1, 0); STA(SA(0, 1), 128, t + 2);
    WAIT_L(8); BAR; WAIT_L(0); MMA(0, 0, At, B0); BAR; SCHED;
    LDB(B1, 1, 1); STB(SB(1, 0), 0, t + 3);
    BAR; WAIT_L(0); MMA(0, 1, At, B1); BAR;
    LDA(At, 1, 1); STA(SA(1, 0), 0, t + 3);
    BAR; WAIT_L(0); MMA(1, 0, At, B0); BAR; SCHED;
    STB(SB(1, 1), 128, t + 3);
    WAIT_V(6); BAR; MMA(1, 1, At, B1); BAR;
  }
  int tz = tid_of(wv); wid = tz >> 6; lane = tz & 63; wr = wid >> 2; wc = wid & 3; fr = lane & 15; fq = lane >> 4;
  { LDB(B0, 0, 0); WAIT_V(0); LDA(At, 0, 0); { int _r, _c; stage_rc(tz * 16, _r, _c); const u32 voa2 = (u32)(_r * lda + _c) * 2u; STA_X(tz, voa2, SA(1, 1), 128, nt - 1); }
    BAR; WAIT_L(0); MMA(0, 0, At, B0); BAR;
    LDB(B1, 0, 1); BAR; WAIT_L(0); MMA(0, 1, At, B1); BAR;
    LDA(At, 0, 1); WAIT_V(4); BAR; WAIT_L(0); MMA(1, 0, At, B0); MMA(1, 1, At, B1); BAR; }
  { LDB(B0, 1, 0); LDA(At, 1, 0); WAIT_V(2); BAR; WAIT_L(0); MMA(0, 0, At, B0); BAR;
    LDB(B1, 1, 1); WAIT_V(0); BAR; WAIT_L(0); MMA(0, 1, At, B1); BAR;
    LDA(At, 1, 1); BAR; WAIT_L(0); MMA(1, 0, At, B0); MMA(1, 1, At, B1); BAR; }
  if (wr == 0) BAR;
  const __amdgpu_buffer_rsrc_t rc = __builtin_amdgcn_make_buffer_rsrc((void*)uni((char*)C), 0, 0x7FFFFFFF, 0x00020000);
#pragma unroll
  for (int ai = 0; ai < 2; ++ai)
#pragma unroll
  for (int bj = 0; bj < 2; ++bj)
#pragma unroll
  for (int m = 0; m < 4; ++m) {
    const int row = ai * 128 + wr * 64 + m * 16 + fr, col = bj * 128 + wc * 32 + fq * 8;
    f32x4 v0 = acc[ai][bj][m][0], v1 = acc[ai][bj][m][1];
    if (MODE >= 2) {
      if (MODE == 2) { const u32x4 xv = *(const u32x4*)(bias + (long)row * ldc + col);
        v0[0] += bf_lo(xv.x); v0[1] += bf_hi(xv.x); v0[2] += bf_lo(xv.y); v0[3] += bf_hi(xv.y); v1[0] += bf_lo(xv.z); v1[1] += bf_hi(xv.z); v1[2] += bf_lo(xv.w); v1[3] += bf_hi(xv.w); }
      __builtin_amdgcn_raw_buffer_store_b128(__builtin_bit_cast(u32x4, v0), rc, (row * ldc + col) * 4, 0, 16);
      __builtin_amdgcn_raw_buffer_store_b128(__builtin_bit_cast(u32x4, v1), rc, (row * ldc + col) * 4 + 16, 0, 16);
    } else {
      if (MODE == 1) { const u32x4 bb = *(const u32x4*)(bias + col);
        v0[0] = mish(v0[0] + bf_lo(bb.x)); v0[1] = mish(v0[1] + bf_hi(bb.x)); v0[2] = mish(v0[2] + bf_lo(bb.y)); v0[3] = mish(v0[3] + bf_hi(bb.y));
        v1[0] = mish(v1[0] + bf_lo(bb.z)); v1[1] = mish(v1[1] + bf_hi(bb.z)); v1[2] = mish(v1[2] + bf_lo(bb.w)); v1[3] = mish(v1[3] + bf_hi(bb.w)); }
      __builtin_amdgcn_raw_buffer_store_b128(u32x4{cvt_pk(v0[0], v0[1]), cvt_pk(v0[2], v0[3]), cvt_pk(v1[0], v1[1]), cvt_pk(v1[2], v1[3])}, rc, (row * ldc + col) * 2, 0, 16);
    }
  }
#undef SA
#undef SB
#undef LDA
#undef LDB
#undef MMA
}
DEV void wait_cnts(const int wv, u32* ctl, int ia, int na, u32 ta, int ib, int nb, u32 tb);
DEV void attn_item(const int wv, const Params& p, int s, int h, char* lds) {
  const int t0 = tid_of(0);
  const int l = t0 & 63, q = l >> 4, i = l & 15;
  const __amdgpu_buffer_rsrc_t rq = __builtin_amdgcn_make_buffer_rsrc((void*)((const u16*)(p.ws + WS_Q) + (size_t)s * 32 * HD + h * 128), 0, 0x7FFFFFFF, 0x00020000);
  const __amdgpu_buffer_rsrc_t rm = __builtin_amdgcn_make_buffer_rsrc((void*)((const u16*)(p.ws + WS_KM) + (size_t)s * 8 * HD + h * 128), 0, 0x7FFFFFFF, 0x00020000);
  const u16* K = (const u16*)(p.ws + WS_K) + (size_t)s * 32 * HD + h * 128;
  const u16* V = (const u16*)(p.ws + WS_V) + (size_t)s * 32 * HD + h * 128;
  u32x4 vv[8]; bf16x8 qf[4][2], kf[4][2];
#pragma unroll
  for (int it = 0; it < 8; ++it) vv[it] = *(const u32x4*)(V + (size_t)(it * 4 + q) * HD + i * 8);
#pragma unroll
  for (int ks = 0; ks < 4; ++ks) {
    const int e = ks * 32 + q * 8;
    qf[ks][0] = asfrag(__builtin_amdgcn_raw_buffer_load_b128(rq, (i * HD + e) * 2, 0, 16)); qf[ks][1] = asfrag(__builtin_amdgcn_raw_buffer_load_b128(rq, ((16 + i) * HD + e) * 2, 0, 16));
    kf[ks][0] = *(const bf16x8*)(K + (size_t)i * HD + e); kf[ks][1] = *(const bf16x8*)(K + (size_t)(16 + i) * HD + e);
  }
  __builtin_amdgcn_sched_barrier(0);
#pragma unroll
  for (int it = 0; it < 8; ++it) { const int row = 16 + it * 4 + q; *(u32x4*)(lds + row * 256 + 16 * (i ^ swzb(row))) = vv[it]; }
#pragma unroll
  for (int it = 2; it < 4; ++it) { const int row = it * 4 + q; *(u32x4*)(lds + row * 256 + 16 * (i ^ swzb(row))) = u32x4{0, 0, 0, 0}; }
  f32x4 c[3][2];
#pragma unroll
  for (int t = 0; t < 3; ++t) c[t][0] = c[t][1] = f32x4{0, 0, 0, 0};
#pragma unroll
  for (int ks = 0; ks < 4; ++ks) { c[1][0] = mfma16(kf[ks][0], qf[ks][0], c[1][0]); c[1][1] = mfma16(kf[ks][0], qf[ks][1], c[1][1]); c[2][1] = mfma16(kf[ks][1], qf[ks][1], c[2][1]); }
  wait_cnts(wv, (u32*)(p.ws + WS_CTL), C_KM + s * 32 + (h - wv), 8, 4, 0, 0, 0);
  __syncthreads();
  {
    u32x4 vm[2]; bf16x8 km[4];
#pragma unroll
    for (int it = 0; it < 2; ++it) vm[it] = __builtin_amdgcn_raw_buffer_load_b128(rm, (4u << 20) + ((it * 4 + q) * HD + i * 8) * 2, 0, 16);
#pragma unroll
    for (int ks = 0; ks < 4; ++ks) km[ks] = asfrag(__builtin_amdgcn_raw_buffer_load_b128(rm, ((i & 7) * HD + ks * 32 + q * 8) * 2, 0, 16));
#pragma unroll
    for (int it = 0; it < 2; ++it) { const int row = it * 4 + q; *(u32x4*)(lds + row * 256 + 16 * (i ^ swzb(row))) = vm[it]; }
#pragma unroll
    for (int ks = 0; ks < 4; ++ks) { c[0][0] = mfma16(km[ks], qf[ks][0], c[0][0]); c[0][1] = mfma16(km[ks], qf[ks][1], c[0][1]); }
  }
  const float sc = 0.08838834764831845f * 1.4426950408889634f, NEG = -3.0e38f;
  u32x4 pt[2], pm[2];
#pragma unroll
  for (int u = 0; u < 2; ++u) {
    float sm[4], s1[4], s2[4]; float mx = NEG;
#pragma unroll
    for (int e = 0; e < 4; ++e) {
      sm[e] = q < 2 ? c[0][u][e] * sc : NEG;
      s1[e] = (u == 1 || 4 * q + e <= i) ? c[1][u][e] * sc : NEG;
      s2[e] = (u == 1 && 4 * q + e <= i) ? c[2][u][e] * sc : NEG;
      mx = fmaxf(mx, fmaxf(sm[e], fmaxf(s1[e], s2[e])));
    }
    mx = red32_max(red16_max(mx));
    float sum = 0;
#pragma unroll
    for (int e = 0; e < 4; ++e) {
      sm[e] = q < 2 ? __builtin_amdgcn_exp2f(sm[e] - mx) : 0.f;
      s1[e] = (u == 1 || 4 * q + e <= i) ? __builtin_amdgcn_exp2f(s1[e] - mx) : 0.f;
      s2[e] = (u == 1 && 4 * q + e <= i) ? __builtin_amdgcn_exp2f(s2[e] - mx) : 0.f;
      sum += sm[e] + s1[e] + s2[e];
    }
    sum = red32_add(red16_add(sum));
    const float inv = 1.f / sum;
    pt[u] = u32x4{cvt_pk(s1[0] * inv, s1[1] * inv), cvt_pk(s1[2] * inv, s1[3] * inv), cvt_pk(s2[0] * inv, s2[1] * inv), cvt_pk(s2[2] * inv, s2[3] * inv)};
    pm[u] = u32x4{cvt_pk(sm[0] * inv, sm[1] * inv), cvt_pk(sm[2] * inv, sm[3] * inv), 0u, 0u};
  }
  asm volatile("s_waitcnt lgkmcnt(0)" ::: "memory");
  u16* O = (u16*)(p.ws + WS_O) + (size_t)s * 32 * HD + h * 128;
  const __amdgpu_buffer_rsrc_t ro = __builtin_amdgcn_make_buffer_rsrc((void*)O, 0, 0x7FFFFFFF, 0x00020000);
  const s16x4 zz = {0, 0, 0, 0};
#pragma unroll
  for (int cp = 0; cp < 4; ++cp) {
    f32x4 o[2][2];
#pragma unroll
    for (int c2 = 0; c2 < 2; ++c2) {
      const int ch = 4 * cp + (i & 3);
      const int rm = 4 * q + (i >> 2), r1 = 16 + rm, r2 = 32 + rm;
      const bf16x8 vm = cat4(trread(lds + rm * 256 + 16 * (ch ^ swzb(rm)) + 8 * c2), zz);
      const bf16x8 vt = cat4(trread(lds + r1 * 256 + 16 * (ch ^ swzb(r1)) + 8 * c2), trread(lds + r2 * 256 + 16 * (ch ^ swzb(r2)) + 8 * c2));
#pragma unroll
      for (int u = 0; u < 2; ++u) o[c2][u] = mfma16(vm, asfrag(pm[u]), mfma16(vt, asfrag(pt[u]), f32x4{0, 0, 0, 0}));
    }
#pragma unroll
    for (int u = 0; u < 2; ++u)
      __builtin_amdgcn_raw_buffer_store_b128(u32x4{cvt_pk(o[0][u][0], o[0][u][1]), cvt_pk(o[0][u][2], o[0][u][3]), cvt_pk(o[1][u][0], o[1][u][1]), cvt_pk(o[1][u][2], o[1][u][3])}, ro, ((16 * u + i) * HD + 32 * cp + 8 * q) * 2, 0, 16);
  }
}

DEV void wo_item(const int wv, const Params& p, int g, int nt_, int kq, char* smem) {
  const int tid = tid_of(wv);
  const int lane = tid & 63, wr = wv >> 2, wc = wv & 3, fr = lane & 15, fq = lane >> 4;
  const __amdgpu_buffer_rsrc_t rsa = __builtin_amdgcn_make_buffer_rsrc((void*)((const u16*)(p.ws + WS_O) + (size_t)g * 128 * HD + kq * 1024), 0, 0x7FFFFFFF, 0x00020000);
  const __amdgpu_buffer_rsrc_t rsb = __builtin_amdgcn_make_buffer_rsrc((void*)(p.Wo + (size_t)kq * 1024 * D_ + nt_ * 128), 0, 0x7FFFFFFF, 0x00020000);
  u32 voa, vob;
  { int r, c; stage_rc(tid * 16, r, c); voa = (u32)(r * HD + c) * 2u; const int b = tid * 16, rb = b >> 8; vob = (u32)(rb * D_ + ((((b >> 4) & 15) ^ swzb(rb)) * 8)) * 2u; }
  constexpr int STG = 32768;
  auto stage = [&](int kt) {
    char* sa = smem + (kt & 3) * STG + tid * 16;
    __builtin_amdgcn_raw_ptr_buffer_load_lds(rsa, (LDSP void*)sa, 16, voa, kt * 128, 0, 16);
    __builtin_amdgcn_raw_ptr_buffer_load_lds(rsa, (LDSP void*)(sa + 8192), 16, voa, kt * 128 + 64 * HD * 2, 0, 16);
    __builtin_amdgcn_raw_ptr_buffer_load_lds(rsb, (LDSP void*)(sa + 16384), 16, vob, kt * 64 * D_ * 2, 0, 0);
    __builtin_amdgcn_raw_ptr_buffer_load_lds(rsb, (LDSP void*)(sa + 24576), 16, vob, (kt * 64 + 32) * D_ * 2, 0, 0);
  };
  int bo[2];
#pragma unroll
  for (int t = 0; t < 2; ++t) { const int row = 8 * fq + 4 * t + (fr >> 2); bo[t] = 16384 + 256 * row + 16 * ((4 * wc + (fr & 3)) ^ swzb(row)); }
  const int ao = lds_byte(wr * 64 + fr, fq * 8);
  f32x4 acc[4][2];
#pragma unroll
  for (int m = 0; m < 4; ++m) acc[m][0] = acc[m][1] = f32x4{0, 0, 0, 0};
  stage(0); stage(1); stage(2);
  constexpr int nt = 16;
  for (int kt = 0; kt < nt; ++kt) {
    if (kt + 2 < nt) asm volatile("s_waitcnt vmcnt(8)" ::: "memory"); else asm volatile("s_waitcnt vmcnt(0)" ::: "memory");
    __builtin_amdgcn_s_barrier();
    if (kt + 3 < nt) stage(kt + 3);
    const char* sa = smem + (kt & 3) * STG;
#pragma unroll
    for (int k = 0; k < 2; ++k) {
      const bf16x8 b0 = cat4(trread(sa + k * 8192 + bo[0]), trread(sa + k * 8192 + bo[1])), b1 = cat4(trread(sa + k * 8192 + bo[0] + 8), trread(sa + k * 8192 + bo[1] + 8));
#pragma unroll
      for (int m = 0; m < 4; ++m) {
        const bf16x8 a = *(const bf16x8*)(sa + ao + m * 2048 + k * 1024);
        acc[m][0] = mfma16(b0, a, acc[m][0]); acc[m][1] = mfma16(b1, a, acc[m][1]);
      }
    }
    asm volatile("s_waitcnt lgkmcnt(0)" ::: "memory");
  }
  char* part = p.ws + WS_P + (size_t)(g * 8 + nt_) * (4 * 65536);
  const __amdgpu_buffer_rsrc_t rp = __builtin_amdgcn_make_buffer_rsrc((void*)part, 0, 0x7FFFFFFF, 0x00020000);
#pragma unroll
  for (int m = 0; m < 4; ++m)
#pragma unroll
    for (int n = 0; n < 2; ++n) __builtin_amdgcn_raw_buffer_store_b128(__builtin_bit_cast(u32x4, acc[m][n]), rp, tid * 16 + (m * 2 + n) * 8192, kq * 65536, 16);
  asm volatile("s_waitcnt vmcnt(0)" ::: "memory");
  __syncthreads();
  volatile int* s_last = (volatile int*)(smem + 128 * 1024 + 32);
  if (tid == 0) *s_last = (int)ctl_add((u32*)(p.ws + WS_CTL) + C_P + g * 8 + nt_, 1);
  __syncthreads();
  if (__builtin_amdgcn_readfirstlane(*s_last) != 3) return;
  if (wv == 0) {
    u32* cy = (u32*)(p.ws + WS_CTL) + C_Y + (g >> 1) * 4 + (nt_ >> 1);
    for (int sp = 0; __builtin_amdgcn_readfirstlane(ctl_ld(cy)) < 2; ++sp) { __builtin_amdgcn_s_sleep(6); if (sp > 400000) { ctl_st((u32*)(p.ws + WS_CTL) + C_TMO, 1); break; } }
  }
  __syncthreads();
  const float* Y1 = (const float*)(p.ws + WS_Y); const float* Y2 = (const float*)(p.ws + WS_Y + (8u << 20));
  const __amdgpu_buffer_rsrc_t ry = __builtin_amdgcn_make_buffer_rsrc((void*)Y1, 0, 0x7FFFFFFF, 0x00020000);
#pragma unroll
  for (int m = 0; m < 4; ++m) {
    const u32 o = (u32)(g * 128 + wr * 64 + m * 16 + fr) * D_ + nt_ * 128 + wc * 32 + fq * 8;
    f32x4 v[2];
#pragma unroll
    for (int n = 0; n < 2; ++n) {
      f32x4 t[4];
#pragma unroll
      for (int k = 0; k < 4; ++k) t[k] = __builtin_bit_cast(f32x4, __builtin_amdgcn_raw_buffer_load_b128(rp, tid * 16 + (m * 2 + n) * 8192, k * 65536, 16));
      const f32x4 y1 = __builtin_bit_cast(f32x4, __builtin_amdgcn_raw_buffer_load_b128(ry, o * 4 + n * 16, 0, 16)), y2 = __builtin_bit_cast(f32x4, __builtin_amdgcn_raw_buffer_load_b128(ry, o * 4 + n * 16, 8u << 20, 16));
      v[n] = ((t[0] + t[1]) + (t[2] + t[3])) + (y1 + y2);
    }
    *(u32x4*)(p.out + o) = u32x4{cvt_pk(v[0][0], v[0][1]), cvt_pk(v[0][2], v[0][3]), cvt_pk(v[1][0], v[1][1]), cvt_pk(v[1][2], v[1][3])};
  }
}
constexpr int LDS_BYTES = 128 * 1024 + 64;
DEV void wait_cnts(const int wv, u32* ctl, int ia, int na, u32 ta, int ib, int nb, u32 tb) {
  if (wv == 0) {
    const int t = tid_of(0);
    u32* c = ctl + (t < na ? ia + t : (t < na + nb ? ib + t - na : ia));
    const u32 target = t < na ? ta : (t < na + nb ? tb : 0u);
    for (int sp = 0;; ++sp) {
      if (__all(ctl_ld(c) >= target)) break;
      __builtin_amdgcn_s_sleep(6);
      if (sp > 400000 || ((sp & 63) == 63 && __builtin_amdgcn_readfirstlane(ctl_ld(ctl + C_TMO)))) { ctl_st(ctl + C_TMO, 1); break; }
    }
  }
}
DEV void acquire_wg(const int wv) {
  if (tid_of(wv) == 0) { __builtin_amdgcn_fence(__ATOMIC_ACQUIRE, "agent"); asm volatile("s_waitcnt vmcnt(0)" ::: "memory"); }
  __syncthreads();
}
DEV void signal_wg(const int wv, u32* c) {
  asm volatile("s_waitcnt vmcnt(0)" ::: "memory");
  __syncthreads();
  if (tid_of(wv) == 0) ctl_add(c, 1);
}
constexpr int N_F1 = 112, N_Q = 128, N_Y = 64, N_G = 16 * 48, I_Q = N_F1, I_Y = I_Q + N_Q, I_G = I_Y + N_Y, N_ITEMS = I_G + N_G;
typedef __attribute__((address_space(4))) const Params* KArg;
DEV void worker(const int wv, const int role, char* smem) {
  volatile int* s_idx = (volatile int*)(smem + 128 * 1024 + 16);
  int qn = 4, tried = 0;
  for (;;) {
    KArg ka = (KArg)__builtin_amdgcn_kernarg_segment_ptr(); asm volatile("" : "+s"(ka));
    Params p; p.x = ka->x; p.mem0 = ka->mem0; p.Wq = ka->Wq; p.Wk = ka->Wk; p.Wv = ka->Wv; p.Wo = ka->Wo; p.w1 = ka->w1; p.b1 = ka->b1; p.w2 = ka->w2; p.out = ka->out; p.ws = ka->ws;
    u32* ctl = (u32*)(p.ws + WS_CTL);
    const u16* XN = (const u16*)(p.ws + WS_XN);
    __syncthreads();
    if (tid_of(wv) == 0) *s_idx = (int)ctl_add(ctl + C_QUEUE + qn * 32, 1);
    __syncthreads();
    int idx = __builtin_amdgcn_readfirstlane(*s_idx);
    if (qn == 4) { if (idx >= I_G) { qn = (role >> 5) & 3; continue; } }
    else {
      if (idx >= 192) { if (++tried == 4) break; qn = (qn + 1) & 3; continue; }
      int g, k;
      if (idx < 4) { g = 0; k = idx; } else { const int u = idx - 4, gg = u / 12, kk = u - gg * 12;
        if (gg < 15) { if (kk < 4) { g = gg + 1; k = kk; } else { g = gg; k = kk; } } else { g = 15; k = kk + 4; } }
      idx = I_G + g * 48 + (k < 4 ? k * 4 + qn : 16 + qn * 8 + (k - 4));
    }
    if (idx < I_G) {
      const u16 *A, *B, *bias; void* C; int lda, ldb, K, ldc, mode; u32* done;
      if (idx < I_Q) { const int mt = idx & 7, n0 = (idx >> 3) * 256;
        A = XN + (size_t)mt * 256 * D_; lda = D_; B = p.w1 + n0; ldb = FF; K = D_; C = (u16*)(p.ws + WS_ACT) + (size_t)mt * 256 * FF + n0; ldc = FF; bias = p.b1 + n0; mode = 1; done = ctl + C_F1 + mt * 32;
      } else if (idx < I_Y) { const int j = idx - I_Q, mt = j >> 4, n0 = (j & 15) * 256;
        A = XN + (size_t)mt * 256 * D_; lda = D_; B = p.Wq + n0; ldb = HD; K = D_; C = (u16*)(p.ws + WS_Q) + (size_t)mt * 256 * HD + n0; ldc = HD; bias = p.b1; mode = 0; done = ctl + C_Q + j;
      } else { const int j = idx - I_Y, kh = j & 1, mt = j >> 3, n0 = ((j >> 1) & 3) * 256;
        wait_cnts(wv, ctl, C_F1 + mt * 32, 1, 14, 0, 0, 0); acquire_wg(wv);
        A = (const u16*)(p.ws + WS_ACT) + (size_t)mt * 256 * FF + kh * 1792; lda = FF; B = p.w2 + (size_t)kh * 1792 * D_ + n0; ldb = D_; K = 1792;
        C = (float*)(p.ws + WS_Y + (size_t)kh * (8u << 20)) + (size_t)mt * 256 * D_ + n0; ldc = D_; bias = p.x + (size_t)mt * 256 * D_ + n0; mode = kh ? 3 : 2; done = ctl + C_Y + (j >> 1);
      }
      const int dn = __builtin_amdgcn_readfirstlane((int)(done - ctl));
      gemm256(wv, __builtin_amdgcn_readfirstlane(mode), A, __builtin_amdgcn_readfirstlane(lda), B, __builtin_amdgcn_readfirstlane(ldb), __builtin_amdgcn_readfirstlane(K), C, __builtin_amdgcn_readfirstlane(ldc), uni(bias), smem);
      KArg kb = (KArg)__builtin_amdgcn_kernarg_segment_ptr(); asm volatile("" : "+s"(kb));
      signal_wg(wv, (u32*)(kb->ws + WS_CTL) + dn);
    } else {
      const int j = idx - I_G, g = j / 48, k = j - g * 48;
      if (k < 16) {
        const int seg = 4 * g + (k >> 2), h0 = 8 * (k & 3);
        wait_cnts(wv, ctl, C_Q + (seg >> 3) * 16 + (h0 >> 1), 4, 1, 0, 0, 0);
        __syncthreads();
        attn_item(wv, p, seg, h0 + wv, smem + wv * 12288);
        signal_wg(wv, ctl + C_A + g * 4 + (k & 3));
      } else {
        const int kq = (k - 16) >> 3, nt_ = (k - 16) & 7;
        wait_cnts(wv, ctl, C_A + g * 4 + kq, 1, 4, 0, 0, 0);
        __syncthreads();
        wo_item(wv, p, g, nt_, kq, smem);
      }
    }
  }
}
__global__ void __launch_bounds__(512, 2) k_kv(Params p) {
  extern __shared__ __attribute__((aligned(16))) char smem[];
  const int b = blockIdx.x, x = b & 7, o = b >> 3;
  const int mt = o & 7, ct = x * 4 + (o >> 3), mat = ct >> 4, n0 = (ct & 15) * 256;
  gemm256(__builtin_amdgcn_readfirstlane(threadIdx.x >> 6), 0, (const u16*)(p.ws + WS_XN) + (size_t)mt * 256 * D_, D_, (mat ? p.Wv : p.Wk) + n0, HD, D_, (u16*)(p.ws + (mat ? WS_V : WS_K)) + (size_t)mt * 256 * HD + n0, HD, p.b1, smem);
}
__global__ void __launch_bounds__(512, 2) k_main(Params p) {
  extern __shared__ __attribute__((aligned(16))) char smem[];
  const int wv = __builtin_amdgcn_readfirstlane(threadIdx.x >> 6);
  const int role = take_role((u32*)(p.ws + WS_CTL), (int*)(smem + 128 * 1024));
  if (role >= 0 && role < 128) chain_role(p, role, smem);
  __syncthreads();
  worker(wv, role, smem);
}
extern "C" void kernel_launch(void* const* d_in, const int* in_sizes, int n_in, void* d_out, int out_size, void* d_ws, size_t ws_size, hipStream_t stream) {
  Params p;
  p.x = (const u16*)d_in[0]; p.mem0 = (const u16*)d_in[1]; p.Wq = (const u16*)d_in[2]; p.Wk = (const u16*)d_in[3]; p.Wv = (const u16*)d_in[4]; p.Wo = (const u16*)d_in[5];
  p.w1 = (const u16*)d_in[8]; p.b1 = (const u16*)d_in[9]; p.w2 = (const u16*)d_in[10]; p.out = (u16*)d_out; p.ws = (char*)d_ws;
  (void)hipFuncSetAttribute((const void*)k_kv, hipFuncAttributeMaxDynamicSharedMemorySize, LDS_BYTES);
  (void)hipFuncSetAttribute((const void*)k_main, hipFuncAttributeMaxDynamicSharedMemorySize, LDS_BYTES);
  k_prep<<<512, 256, 0, stream>>>(p);
  k_kv<<<256, 512, LDS_BYTES, stream>>>(p);
  k_main<<<256, 512, LDS_BYTES, stream>>>(p);
}
```

```cpp
#include <hip/hip_runtime.h>
#include <hip/hip_bf16.h>
#include <stdint.h>

typedef unsigned int u32;
typedef unsigned short u16;
typedef u32 u32x4 __attribute__((ext_vector_type(4)));
typedef u32 u32x2 __attribute__((ext_vector_type(2)));
typedef short bf16x8 __attribute__((ext_vector_type(8)));
typedef short s16x4 __attribute__((ext_vector_type(4)));
typedef float f32x4 __attribute__((ext_vector_type(4)));
typedef __bf16 bf2_t __attribute__((ext_vector_type(2)));
typedef float f32x2 __attribute__((ext_vector_type(2)));
#define LDSP __attribute__((address_space(3)))
#define DEV __device__ __forceinline__

constexpr int S_ = 2048, D_ = 1024, HD = 4096, FF = 3584, NSEG = 64;
constexpr u32 SENT = 0xFFFFFFFFu;
constexpr size_t WS_XN = 0;
constexpr size_t WS_Q = 4u << 20;
constexpr size_t WS_K = 20u << 20;
constexpr size_t WS_V = 36u << 20;
constexpr size_t WS_ACT = 52u << 20;
constexpr size_t WS_O = 68u << 20;
constexpr size_t WS_KM = 84u << 20;
constexpr size_t WS_VM = 88u << 20;
constexpr size_t WS_CTL = 92u << 20;
constexpr size_t WS_XCH = 96u << 20;
constexpr size_t WS_Y = 100u << 20;
constexpr size_t WS_P = 116u << 20;
constexpr u32 SLOT = 256 * 1024, X_M = 0, X_O = 32768, X_SC = 98304;
constexpr int C_TICK = 0;
constexpr int C_QUEUE = 1408  , C_F1 = 512, C_Q = C_F1 + 8 * 32, C_Y = C_Q + 128, C_A = C_Y + 32  , C_P = C_A + 128  , C_KV = 1600  , C_KM = 2048;
constexpr int C_OVF = 8 * 32, C_TOTAL = 9 * 32, C_NEAR = 10 * 32, C_TMO = 11 * 32;

DEV u32 cvt_pk(float a, float b) { f32x2 v = {a, b}; bf2_t r = __builtin_convertvector(v, bf2_t); return __builtin_bit_cast(u32, r); }
DEV float bf_lo(u32 x) { return __uint_as_float(x << 16); }
DEV float bf_hi(u32 x) { return __uint_as_float(x & 0xFFFF0000u); }
DEV u32 nosent(u32 x) { return x == SENT ? 0xFFFEFFFFu : x; }
DEV float ror8(float x) { return __int_as_float(__builtin_amdgcn_update_dpp(0, __float_as_int(x), 0x128, 0xF, 0xF, true)); }
DEV u32 ror8u(u32 x) { return (u32)__builtin_amdgcn_update_dpp(0, (int)x, 0x128, 0xF, 0xF, true); }
DEV float xor16(float x) { u32 a = __float_as_uint(x); auto r = __builtin_amdgcn_permlane16_swap(a, a, false, false); return __uint_as_float((threadIdx.x & 16) ? r[0] : r[1]); }
DEV float xor32(float x) { u32 a = __float_as_uint(x); auto r = __builtin_amdgcn_permlane32_swap(a, a, false, false); return __uint_as_float((threadIdx.x & 32) ? r[0] : r[1]); }
DEV f32x4 mfma16(bf16x8 a, bf16x8 b, f32x4 c) { return __builtin_amdgcn_mfma_f32_16x16x32_bf16(a, b, c, 0, 0, 0); }
DEV bf16x8 asfrag(u32x4 v) { return __builtin_bit_cast(bf16x8, v); }
DEV s16x4 trread(const char* p) { return __builtin_amdgcn_ds_read_tr16_b64_v4i16((LDSP s16x4*)p); }
DEV bf16x8 cat4(s16x4 a, s16x4 b) { return __builtin_shufflevector(a, b, 0, 1, 2, 3, 4, 5, 6, 7); }
DEV bool valid4(u32x4 v) { return (v.x != SENT) & (v.y != SENT) & (v.z != SENT) & (v.w != SENT); }
DEV u32x4 ld_sc1(__amdgpu_buffer_rsrc_t rs, u32 off) { return __builtin_amdgcn_raw_buffer_load_b128(rs, off, 0, 16); }
DEV void st_sc1(__amdgpu_buffer_rsrc_t rs, u32 off, u32x4 v) { __builtin_amdgcn_raw_buffer_store_b128(v, rs, off, 0, 16); }
DEV u32 ctl_ld(u32* p) { return __hip_atomic_load(p, __ATOMIC_RELAXED, __HIP_MEMORY_SCOPE_AGENT); }
DEV void ctl_st(u32* p, u32 v) { __hip_atomic_store(p, v, __ATOMIC_RELAXED, __HIP_MEMORY_SCOPE_AGENT); }
DEV u32 ctl_add(u32* p, u32 v) { return __hip_atomic_fetch_add(p, v, __ATOMIC_RELAXED, __HIP_MEMORY_SCOPE_AGENT); }
DEV char* xch_base(char* ws) { return (char*)(((size_t)(ws + WS_XCH) + (2u << 20) - 1) & ~(size_t)((2u << 20) - 1)); }

DEV int tid_of(int wv) { int ln; asm volatile("v_mbcnt_lo_u32_b32 %0, -1, 0\n v_mbcnt_hi_u32_b32 %0, -1, %0" : "=v"(ln)); return wv * 64 + ln; }
template <class T> DEV T* uni(T* p) {
  const unsigned long long a = (unsigned long long)p;
  return (T*)(((unsigned long long)(u32)__builtin_amdgcn_readfirstlane((int)(a >> 32)) << 32) | (u32)__builtin_amdgcn_readfirstlane((int)a));
}

struct Params {
  const u16* x; const u16* mem0; const u16* Wq; const u16* Wk; const u16* Wv; const u16* Wo;
  const u16* w1; const u16* b1; const u16* w2; u16* out; char* ws;
};

__global__ void __launch_bounds__(256) k_prep(Params p) {
  const int tid = threadIdx.x, lane = tid & 63, wave = tid >> 6, b = blockIdx.x;
  {
    const int row = b * 4 + wave;
    const u32x4* src = (const u32x4*)(p.x + (size_t)row * D_);
    u32x4 v0 = src[lane], v1 = src[lane + 64];
    float f[16];
    f[0] = bf_lo(v0.x); f[1] = bf_hi(v0.x); f[2] = bf_lo(v0.y); f[3] = bf_hi(v0.y); f[4] = bf_lo(v0.z); f[5] = bf_hi(v0.z); f[6] = bf_lo(v0.w); f[7] = bf_hi(v0.w);
    f[8] = bf_lo(v1.x); f[9] = bf_hi(v1.x); f[10] = bf_lo(v1.y); f[11] = bf_hi(v1.y); f[12] = bf_lo(v1.z); f[13] = bf_hi(v1.z); f[14] = bf_lo(v1.w); f[15] = bf_hi(v1.w);
    float s = 0; for (int i = 0; i < 16; ++i) s += f[i];
    for (int o = 32; o; o >>= 1) s += __shfl_xor(s, o);
    const float mean = s * (1.f / D_);
    float q = 0; for (int i = 0; i < 16; ++i) { f[i] -= mean; q += f[i] * f[i]; }
    for (int o = 32; o; o >>= 1) q += __shfl_xor(q, o);
    const float rs = 1.f / sqrtf(q * (1.f / D_) + 1e-5f);
    u32x4 o0 = {cvt_pk(f[0] * rs, f[1] * rs), cvt_pk(f[2] * rs, f[3] * rs), cvt_pk(f[4] * rs, f[5] * rs), cvt_pk(f[6] * rs, f[7] * rs)};
    u32x4 o1 = {cvt_pk(f[8] * rs, f[9] * rs), cvt_pk(f[10] * rs, f[11] * rs), cvt_pk(f[12] * rs, f[13] * rs), cvt_pk(f[14] * rs, f[15] * rs)};
    u32x4* dst = (u32x4*)(p.ws + WS_XN + (size_t)row * D_ * 2);
    dst[lane] = o0; dst[lane + 64] = o1;
  }
  {
    const u32 off = (u32)(b * 256 + tid) * 16;
    const u32 x = ((off >> 14) << 13) | (off & 8191);
    u32x4 v = {SENT, SENT, SENT, SENT};
    if (x < 32768) {
      const u32 c8 = x >> 8, g = (x >> 7) & 1, r = (x >> 4) & 7;
      if (g == 0) { v = *(const u32x4*)(p.mem0 + r * D_ + c8 * 8); v.x = nosent(v.x); v.y = nosent(v.y); v.z = nosent(v.z); v.w = nosent(v.w); }
      else v = u32x4{0, 0, 0, 0};
    }
    *(u32x4*)(xch_base(p.ws) + off) = v;
  }
  if (b == 0) { u32* ctl = (u32*)(p.ws + WS_CTL); for (int i = tid; i < 4096; i += 256) ctl[i] = 0; }
}

DEV int take_role(u32* ctl, int* s_tmp) {
  if (threadIdx.x == 0) {
    u32 xcc; asm volatile("s_getreg_b32 %0, hwreg(HW_REG_XCC_ID)" : "=s"(xcc)); xcc &= 7;
    const u32 t = ctl_add(ctl + C_TICK + xcc * 32, 1);
    int role;
    if (t < 32) { role = xcc * 32 + t; ctl_add(ctl + C_TOTAL, 1); }
    else {
      const u32 k = ctl_add(ctl + C_OVF, 1); ctl_add(ctl + C_TOTAL, 1);
      int sp = 0; while (ctl_ld(ctl + C_TOTAL) < gridDim.x && ++sp < 2000000) __builtin_amdgcn_s_sleep(2);
      role = -1; u32 n = 0;
      for (int xx = 0; xx < 8; ++xx) { const u32 c = ctl_ld(ctl + C_TICK + xx * 32); if (c < 32) { if (k < n + (32 - c)) { role = xx * 32 + c + (k - n); break; } n += 32 - c; } }
    }
    *s_tmp = role;
  }
  __syncthreads();
  return __builtin_amdgcn_readfirstlane(*s_tmp);
}
DEV float red16_add(float x) { u32 a = __float_as_uint(x); auto r = __builtin_amdgcn_permlane16_swap(a, a, false, false); return __uint_as_float(r[0]) + __uint_as_float(r[1]); }
DEV float red32_add(float x) { u32 a = __float_as_uint(x); auto r = __builtin_amdgcn_permlane32_swap(a, a, false, false); return __uint_as_float(r[0]) + __uint_as_float(r[1]); }
DEV float red16_max(float x) { u32 a = __float_as_uint(x); auto r = __builtin_amdgcn_permlane16_swap(a, a, false, false); return fmaxf(__uint_as_float(r[0]), __uint_as_float(r[1])); }
DEV float red32_max(float x) { u32 a = __float_as_uint(x); auto r = __builtin_amdgcn_permlane32_swap(a, a, false, false); return fmaxf(__uint_as_float(r[0]), __uint_as_float(r[1])); }
DEV f32x4 halfsum4(f32x4 a, f32x4 b) {
  float d0, d1, d2, d3;
  asm volatile("s_nop 7\n s_nop 3\n"
               "v_add_f32_dpp %0, %4, %4 row_ror:8 row_mask:0xf bank_mask:0x3\n v_add_f32_dpp %1, %5, %5 row_ror:8 row_mask:0xf bank_mask:0x3\n"
               "v_add_f32_dpp %2, %6, %6 row_ror:8 row_mask:0xf bank_mask:0x3\n v_add_f32_dpp %3, %7, %7 row_ror:8 row_mask:0xf bank_mask:0x3\n"
               "v_add_f32_dpp %0, %8, %8 row_ror:8 row_mask:0xf bank_mask:0xc\n v_add_f32_dpp %1, %9, %9 row_ror:8 row_mask:0xf bank_mask:0xc\n"
               "v_add_f32_dpp %2, %10, %10 row_ror:8 row_mask:0xf bank_mask:0xc\n v_add_f32_dpp %3, %11, %11 row_ror:8 row_mask:0xf bank_mask:0xc\n s_nop 1"
               : "=&v"(d0), "=&v"(d1), "=&v"(d2), "=&v"(d3)
               : "v"(a[0]), "v"(a[1]), "v"(a[2]), "v"(a[3]), "v"(b[0]), "v"(b[1]), "v"(b[2]), "v"(b[3]));
  return f32x4{d0, d1, d2, d3};
}
DEV u32 nos(u32 x) { return min(x, 0xFFFFFFFEu); }
#define BARL() asm volatile("s_waitcnt lgkmcnt(0)\n s_barrier" ::: "memory")
DEV void nap(int n) { for (int i = 0; i < n; ++i) __builtin_amdgcn_s_sleep(1); }
#define CH_D1 12
#define CH_D3 10
#define CH_D2 8
#define SMW 2
#define CH_INC 1
constexpr int SPIN_CAP = 150000;
constexpr int L_RQK = 0;
constexpr int L_RV = 8 * 584 * 4;
constexpr int L_R2 = L_RV + 8 * 296 * 4;
constexpr int L_KT = L_R2 + 8 * 160 * 4;
constexpr int L_VT = L_KT + 2 * 32 * 64;
constexpr int L_VM = L_VT + 2 * 32 * 64;
constexpr int L_F = L_VM + 16 * 64 + 64;
constexpr int L_W2 = L_F + 1024;
constexpr int L_CHAIN_END = L_VM + 16 * 64;

#define POLL(N, V, LDEXPR, NSP) do { \
    if (dead) { _Pragma("unroll") for (int k = 0; k < N; ++k) V[k] = u32x4{0, 0, 0, 0}; } \
    else for (int sp = 0;; ++sp) { \
      asm volatile("" ::: "memory"); \
      _Pragma("unroll") for (int k = 0; k < N; ++k) V[k] = (LDEXPR); \
      u32 mxv = 0; \
      _Pragma("unroll") for (int k = 0; k < N; ++k) mxv = max(max(mxv, max(V[k].x, V[k].y)), max(V[k].z, V[k].w)); \
      if (__all(mxv != SENT)) { NSP += sp; break; } \
      if (sp > SPIN_CAP || ((sp & 255) == 255 && __builtin_amdgcn_readfirstlane(ctl_ld(ctl + C_TMO)))) { ctl_st(ctl + C_TMO, 1); dead = true; break; } \
    } } while (0)
DEV u32x4 ldx(__amdgpu_buffer_rsrc_t rs, u32 voff, u32 soff) { return __builtin_amdgcn_raw_buffer_load_b128(rs, voff, soff, 16); }
DEV void stx(__amdgpu_buffer_rsrc_t rs, u32 voff, u32 soff, u32x4 v) { __builtin_amdgcn_raw_buffer_store_b128(v, rs, voff, soff, 16); }

DEV void chain_role(const Params& p, const int role, char* smem) {
  const int tid = threadIdx.x, w = __builtin_amdgcn_readfirstlane(tid >> 6), l = tid & 63, q = l >> 4, i = l & 15, r = l & 7, g2 = (l >> 3) & 1;
  const int h = role >> 2, j = role & 3, cs = role >> 1, g = role & 1;
  u32* ctl = (u32*)(p.ws + WS_CTL);
  char* xb = xch_base(p.ws);
  if (role == 0 && w == 0) {
    u32 lat[2] = {1u << 30, 1u << 30};
    for (int rep = 0; rep < 24; ++rep) {
      const int c = rep & 1;
      u32* pq = (u32*)(xb + 127 * 16384 + c * 8192 + 8064) + (l & 31);
      const long long t0 = clock64();
      asm volatile("global_store_dword %0, %1, off sc1\n s_waitcnt vmcnt(0)" ::"v"(pq), "v"(SENT) : "memory");
      const u32 dt = (u32)(clock64() - t0);
      if (c) lat[1] = min(lat[1], dt); else lat[0] = min(lat[0], dt);
    }
    if (l == 0) { ctl[C_NEAR + 1] = lat[0]; ctl[C_NEAR + 2] = lat[1]; ctl_st(ctl + C_NEAR, lat[1] < lat[0] ? 2u : 1u); }
  }
  bf16x8 W1[6][4];
  bf16x8 W2t[8];
  {
    char* stg = smem + w * 8192;
    const u16* mats[3] = {p.Wq, p.Wk, p.Wv};
    u32x4 raw[4][8];
#pragma unroll
    for (int m = 0; m < 3; ++m) {
      const u16* src = mats[m] + (size_t)(128 * w) * HD + h * 128 + 32 * j;
#pragma unroll
      for (int it = 0; it < 8; ++it) raw[m][it] = *(const u32x4*)(src + (size_t)(it * 16 + (l >> 2)) * HD + (l & 3) * 8);
    }
    {
      const u16* src = p.Wo + (size_t)(2048 * g + 256 * w) * D_ + 16 * cs;
#pragma unroll
      for (int it = 0; it < 8; ++it) raw[3][it] = *(const u32x4*)(src + (size_t)(it * 32 + (l >> 1)) * D_ + (l & 1) * 8);
    }
    __builtin_amdgcn_sched_barrier(0);
#pragma unroll
    for (int m = 0; m < 3; ++m) {
#pragma unroll
      for (int it = 0; it < 8; ++it) *(u32x4*)(stg + (it * 16 + (l >> 2)) * 64 + (l & 3) * 16) = raw[m][it];
      asm volatile("s_waitcnt lgkmcnt(0)" ::: "memory");
#pragma unroll
      for (int t = 0; t < 2; ++t)
#pragma unroll
        for (int ks = 0; ks < 4; ++ks) {
          const char* a = stg + (32 * ks + 8 * q + (i >> 2)) * 64 + t * 32 + (i & 3) * 8;
          W1[m * 2 + t][ks] = cat4(trread(a), trread(a + 4 * 64));
        }
      asm volatile("s_waitcnt lgkmcnt(0)" ::: "memory");
    }
#pragma unroll
    for (int it = 0; it < 8; ++it) *(u32x4*)(stg + (it * 32 + (l >> 1)) * 32 + (l & 1) * 16) = raw[3][it];
    asm volatile("s_waitcnt lgkmcnt(0)" ::: "memory");
#pragma unroll
    for (int ks = 0; ks < 8; ++ks) { const char* a = stg + (32 * ks + 8 * q + (i >> 2)) * 32 + (i & 3) * 8; W2t[ks] = cat4(trread(a), trread(a + 4 * 32)); }
    asm volatile("s_waitcnt lgkmcnt(0)" ::: "memory");
  }
  __syncthreads();
#pragma unroll
  for (int ks = 0; ks < 8; ++ks) *(bf16x8*)(smem + L_W2 + (w * 8 + ks) * 1024 + l * 16) = W2t[ks];
  const unsigned long long kvb = (unsigned long long)uni((const u16*)(p.ws + WS_K) + h * 128 + 32 * j);
  const u32x4 rkv = {(u32)__builtin_amdgcn_readfirstlane((int)kvb), (u32)__builtin_amdgcn_readfirstlane((int)(kvb >> 32)), 0x7FFFFFFFu, 0x00020000u};
  const u32 vkv = (u32)((l >> 2) * HD + (l & 3) * 8) * 2u;
  const u32 ldsk = (u32)(size_t)(LDSP char*)(smem + L_KT);
  auto dma = [&](u32 dst, u32 so) { asm volatile("s_mov_b32 m0, %0\n s_nop 0\n buffer_load_dwordx4 %1, %2, %3 offen sc1 lds" ::"s"(dst), "v"(vkv), "s"(rkv), "s"(so) : "memory"); };
  auto prefetch = [&](int s) {
    if ((s & 7) == 0) {
      u32* c = ctl + C_KV + (s >> 3) * 32;
      for (int sp = 0; __builtin_amdgcn_readfirstlane(ctl_ld(c)) < (s ? 32u : 128u); ++sp) { __builtin_amdgcn_s_sleep(4); if (sp > 1000000) { ctl_st(ctl + C_TMO, 1); break; } }
    }
    const u32 d = ldsk + (s & 1) * 2048, so = (u32)s * 32 * HD * 2;
    dma(d, so); dma(d + 1024, so + 16 * HD * 2); dma(d + 4096, so + (u32)(WS_V - WS_K)); dma(d + 4096 + 1024, so + (u32)(WS_V - WS_K) + 16 * HD * 2);
  };
  if (w == 7) { prefetch(0); asm volatile("s_waitcnt vmcnt(0)" ::: "memory"); }
  if (w == 0 && l < 32) *(u32x4*)(smem + L_VM + 512 + l * 16) = u32x4{0, 0, 0, 0};
  if (tid == 0) { int sp = 0; u32 c; while ((c = ctl_ld(ctl + C_NEAR)) == 0 && ++sp < 4000000) __builtin_amdgcn_s_sleep(1); *(volatile u32*)(smem + L_CHAIN_END) = c; }
  __syncthreads();
  const u32 near = (__builtin_amdgcn_readfirstlane(*(volatile u32*)(smem + L_CHAIN_END)) == 2) ? 8192u : 0u;
  const __amdgpu_buffer_rsrc_t rs = __builtin_amdgcn_make_buffer_rsrc((void*)(xb + near), 0, 0x7FFFFFFF, 0x00020000);
  auto PH = [](u32 x) -> u32 { return ((x >> 13) << 14) | (x & 8191); };
  constexpr u32 X_S0 = X_SC + 131072;
  const u32 oM = PH(X_M + 4 * w * 1024 + l * 16), oO = PH(X_O + g * 32768 + (32 * w + 4 * g2 + q) * 128 + r * 16);
  const u32 oS = PH(X_SC + h * 4096 + g2 * 512 + q * 128 + r * 16);
  const u32 oS0 = PH(X_S0 + h * 1024 + ((q >> 1) * 2 + g2) * 256 + (q & 1) * 128 + r * 16);
  const u32 pS0 = PH(X_S0 + h * 1024 + j * 256 + (q & 1) * 128 + r * 16);
  const u32 pO = PH(X_O + role * 512 + q * 128 + r * 16);
  const u32 pM = PH(X_M + (2 * cs + ((l >> 2) & 1)) * 256 + g * 128 + (l >> 3) * 16);
  const u32 jo = j * 1024;
  float* RQK = (float*)(smem + L_RQK); float* RV = (float*)(smem + L_RV); float* R2 = (float*)(smem + L_R2);
  const __amdgpu_buffer_rsrc_t rkm = __builtin_amdgcn_make_buffer_rsrc((void*)((u16*)(p.ws + WS_KM) + h * 128 + 32 * j), 0, 0x7FFFFFFF, 0x00020000);
  bool dead = false; int ns1 = 0, ns2 = 0, ns3 = 0;
  int d1 = (w == 1 ? CH_D1 - 4 : CH_D1), d2 = CH_D2, d3 = CH_D3;
#define TS(k)
  const u32x4 SV = {SENT, SENT, SENT, SENT};

  auto reset = [&](int s) {
    const u32 so = (u32)((s - 2) & 3) * (2 * SLOT);
    stx(rs, oS, so + jo, SV); if (l < 32 && g2 == 0) stx(rs, pS0, so, SV);
    if (g2 == 0) stx(rs, pO, so, SV);
    if ((l & 3) == 0) stx(rs, pM, so, SV);
  };
#define RESET() do { if (w == 6 && s >= 2) reset(s); } while (0)
#define PFPOS 3
#define RSPOS 0
  for (int s = 0; s < NSEG; ++s) {
    const u32 sb = (u32)(s & 3) * (2 * SLOT);
    u32x4 mv[4];
    if (s) nap(d1);
    TS(0);
    f32x4 acc[6];
    const f32x4 z4 = {0, 0, 0, 0};
    {
#pragma unroll
      for (int k = 0; k < 4; ++k) mv[k] = ldx(rs, oM + k * 1024, sb);
      if (PFPOS == 4 && w == 7 && s + 1 < NSEG) prefetch(s + 1);
#pragma unroll
      for (int ks = 0; ks < 4; ++ks)
#pragma unroll
        for (int t = 0; t < 4; ++t) acc[t] = mfma16(W1[t][ks], asfrag(mv[ks]), ks ? acc[t] : z4);
      u32 mxv = 0;
#pragma unroll
      for (int k = 0; k < 4; ++k) mxv = max(max(mxv, max(mv[k].x, mv[k].y)), max(mv[k].z, mv[k].w));
      if (!__all(mxv != SENT)) {
        ++ns1; d1 = min(d1 + CH_INC, CH_D1 + 10);
        POLL(4, mv, ldx(rs, oM + k * 1024, sb), ns1);
#pragma unroll
        for (int ks = 0; ks < 4; ++ks)
#pragma unroll
          for (int t = 0; t < 4; ++t) acc[t] = mfma16(W1[t][ks], asfrag(mv[ks]), ks ? acc[t] : z4);
      }
    }
    TS(1);
    {
      f32x4 a, b;
      a = halfsum4(acc[0], acc[2]); b = halfsum4(acc[1], acc[3]);
      float* d = RQK + w * 584 + g2 * 288 + r * 36 + 4 * q;
      *(f32x4*)d = a; *(f32x4*)(d + 16) = b;
    }
    BARL();
    TS(2);
    if (PFPOS == 0 && w == 7 && s + 1 < NSEG) prefetch(s + 1);
    if (RSPOS == 0) RESET();
    {
      const float* b = RQK + (l >> 5) * 288 + w * 36 + (l & 31);
      float v[8];
#pragma unroll
      for (int ww = 0; ww < 8; ++ww) v[ww] = b[ww * 584];
      __builtin_amdgcn_sched_barrier(0);
      const float t = ((v[0] + v[1]) + (v[2] + v[3])) + ((v[4] + v[5]) + (v[6] + v[7]));
      const float o = __int_as_float(__builtin_amdgcn_update_dpp(0, __float_as_int(t), 0xB1, 0xF, 0xF, true));
      if ((l & 1) == 0) *(u32*)(smem + L_F + (l >> 5) * 512 + w * 64 + (l & 31) * 2) = cvt_pk(t, o);
    }
    BARL();
    if (PFPOS == 3 && w == 7 && s + 1 < NSEG) prefetch(s + 1);
    if (RSPOS == 3) RESET();
    if (w == 0) {
      const u32x4 qf = *(const u32x4*)(smem + L_F + r * 64 + q * 16);
      const u32x4 kfrag = *(const u32x4*)(smem + L_F + 512 + r * 64 + q * 16);
      const char* kt = smem + L_KT + (s & 1) * 2048 + i * 64 + q * 16;
      const bf16x8 kt0 = *(const bf16x8*)kt, kt1 = *(const bf16x8*)(kt + 1024);
      const f32x4 z = {0, 0, 0, 0};
      const f32x4 c1 = mfma16(kt0, asfrag(qf), z);
      const f32x4 c2 = mfma16(kt1, asfrag(qf), z);
      const f32x4 c0 = mfma16(asfrag(kfrag), asfrag(qf), z);
      if (s < NSEG - 1) {
        const u32x4 v12 = {nos(__float_as_uint(g2 ? c2[0] : c1[0])), nos(__float_as_uint(g2 ? c2[1] : c1[1])), nos(__float_as_uint(g2 ? c2[2] : c1[2])), nos(__float_as_uint(g2 ? c2[3] : c1[3]))};
        stx(rs, oS, sb + jo, v12);
        const u32x4 v0 = {nos(__float_as_uint(c0[0])), nos(__float_as_uint(c0[1])), nos(__float_as_uint(c0[2])), nos(__float_as_uint(c0[3]))};
        if (l < 32 && g2 == 0) stx(rs, pS0, sb, v0);
      }
      if (g2 == 0) __builtin_amdgcn_raw_buffer_store_b128(kfrag, rkm, (r * HD + 8 * q) * 2, s * 8 * HD * 2, 16);
    }
    TS(3);
#pragma unroll
    for (int ks = 0; ks < 4; ++ks) { acc[4] = mfma16(W1[4][ks], asfrag(mv[ks]), ks ? acc[4] : z4); acc[5] = mfma16(W1[5][ks], asfrag(mv[ks]), ks ? acc[5] : z4); }
    {
      f32x4 a;
      a = halfsum4(acc[4], acc[5]);
      *(f32x4*)(RV + w * 296 + r * 36 + 16 * g2 + 4 * q) = a;
    }
    u32x4 sv[5];
    if (w == SMW && s < NSEG - 1) {
      nap(d2);
#pragma unroll
      for (int k = 0; k < 5; ++k) sv[k] = k < 4 ? ldx(rs, oS + k * 1024, sb) : ldx(rs, oS0, sb);
    }
    BARL();
    if (PFPOS == 2 && w == 7 && s + 1 < NSEG) prefetch(s + 1);
    if (RSPOS == 2) RESET();
    TS(4);
    if (w == SMW) {
      f32x4 rd[4][2];
#pragma unroll
      for (int ww = 0; ww < 4; ++ww) { const float* b = RV + (4 * g2 + ww) * 296 + r * 36 + 8 * q; rd[ww][0] = *(const f32x4*)b; rd[ww][1] = *(const f32x4*)(b + 4); }
      __builtin_amdgcn_sched_barrier(0);
      f32x4 s0 = (rd[0][0] + rd[1][0]) + (rd[2][0] + rd[3][0]), s1 = (rd[0][1] + rd[1][1]) + (rd[2][1] + rd[3][1]);
#pragma unroll
      for (int e = 0; e < 4; ++e) { s0[e] += ror8(s0[e]); s1[e] += ror8(s1[e]); }
      const u32x4 vf = {cvt_pk(s0[0], s0[1]), cvt_pk(s0[2], s0[3]), cvt_pk(s1[0], s1[1]), cvt_pk(s1[2], s1[3])};
      if (g2 == 0) *(u32x4*)(smem + L_VM + r * 64 + q * 16) = vf;
      if (s < NSEG - 1) {
        const char* vt = smem + L_VT + (s & 1) * 2048 + (4 * q + (i >> 2)) * 64 + (i & 3) * 16;
        const char* vm = smem + L_VM + (4 * q + (i >> 2)) * 64 + (i & 3) * 16;
        const bf16x8 vt0 = cat4(trread(vt), trread(vt + 1024)), vt1 = cat4(trread(vt + 8), trread(vt + 1024 + 8));
        const s16x4 zz = {0, 0, 0, 0};
        const bf16x8 vm0 = cat4(trread(vm), zz), vm1 = cat4(trread(vm + 8), zz);
        TS(5);
        {
          u32 mxv = 0;
#pragma unroll
          for (int k = 0; k < 5; ++k) mxv = max(max(mxv, max(sv[k].x, sv[k].y)), max(sv[k].z, sv[k].w));
          if (!__all(mxv != SENT)) { ++ns2; d2 = min(d2 + CH_INC, CH_D2 + 10); POLL(5, sv, (k < 4 ? ldx(rs, oS + k * 1024, sb) : ldx(rs, oS0, sb)), ns2); }
        }
        TS(6);
        const float sc = 0.08838834764831845f * 1.4426950408889634f;
        float st[4], sm[4];
        st[0] = (__uint_as_float(sv[0].x) + __uint_as_float(sv[1].x)) + (__uint_as_float(sv[2].x) + __uint_as_float(sv[3].x));
        st[1] = (__uint_as_float(sv[0].y) + __uint_as_float(sv[1].y)) + (__uint_as_float(sv[2].y) + __uint_as_float(sv[3].y));
        st[2] = (__uint_as_float(sv[0].z) + __uint_as_float(sv[1].z)) + (__uint_as_float(sv[2].z) + __uint_as_float(sv[3].z));
        st[3] = (__uint_as_float(sv[0].w) + __uint_as_float(sv[1].w)) + (__uint_as_float(sv[2].w) + __uint_as_float(sv[3].w));
        sm[0] = __uint_as_float(sv[4].x); sm[1] = __uint_as_float(sv[4].y); sm[2] = __uint_as_float(sv[4].z); sm[3] = __uint_as_float(sv[4].w);
#pragma unroll
        for (int e = 0; e < 4; ++e) sm[e] = red32_add(sm[e] + ror8(sm[e]));
        float mx = fmaxf(fmaxf(fmaxf(st[0], st[1]), fmaxf(st[2], st[3])), fmaxf(fmaxf(sm[0], sm[1]), fmaxf(sm[2], sm[3])));
        mx = fmaxf(mx, ror8(mx)); mx = red32_max(red16_max(mx));
        const float nm = -mx * sc;
#pragma unroll
        for (int e = 0; e < 4; ++e) { st[e] = __builtin_amdgcn_exp2f(fmaf(st[e], sc, nm)); sm[e] = __builtin_amdgcn_exp2f(fmaf(sm[e], sc, nm)); }
        const u32 p0 = cvt_pk(st[0], st[1]), p1 = cvt_pk(st[2], st[3]);
        const u32 m0 = cvt_pk(sm[0], sm[1]), m1 = cvt_pk(sm[2], sm[3]);
        const u32x4 pt = {p0, p1, ror8u(p0), ror8u(p1)};
        const u32x4 pm = {q < 2 ? m0 : 0u, q < 2 ? m1 : 0u, 0u, 0u};
        const f32x4 z = {0, 0, 0, 0};
        const f32x4 o0 = mfma16(vm0, asfrag(pm), mfma16(vt0, asfrag(pt), z)), o1 = mfma16(vm1, asfrag(pm), mfma16(vt1, asfrag(pt), z));
        float ts = (bf_lo(p0) + bf_hi(p0)) + (bf_lo(p1) + bf_hi(p1)), ms = (bf_lo(m0) + bf_hi(m0)) + (bf_lo(m1) + bf_hi(m1));
        ts += ror8(ts); ts = red32_add(red16_add(ts)); ms = red16_add(ms);
        const float inv = __builtin_amdgcn_rcpf(ts + ms);
        const u32x4 ov = {nos(cvt_pk(o0[0] * inv, o0[1] * inv)), nos(cvt_pk(o0[2] * inv, o0[3] * inv)), nos(cvt_pk(o1[0] * inv, o1[1] * inv)), nos(cvt_pk(o1[2] * inv, o1[3] * inv))};
        if (g2 == 0) stx(rs, pO, sb, ov);
      }
      if (g2 == 0) __builtin_amdgcn_raw_buffer_store_b128(vf, rkm, (4u << 20) + (r * HD + 8 * q) * 2, s * 8 * HD * 2, 16);
    }
    TS(7);
    if (s == NSEG - 1) {
      asm volatile("s_waitcnt vmcnt(0)" ::: "memory");
      BARL();
      if (tid == 0) ctl_add(ctl + C_KM + s * 32 + h, 1);
      break;
    }
    BARL();
    if (PFPOS == 1 && w == 7) prefetch(s + 1);
    if (RSPOS == 1) RESET();
    u32x4 ov[4];
    bf16x8 W2[8];
#pragma unroll
    for (int ks = 0; ks < 8; ++ks) W2[ks] = *(const bf16x8*)(smem + L_W2 + (w * 8 + ks) * 1024 + l * 16);
    nap(d3); TS(8);
    f32x4 a0, a1;
    {
#pragma unroll
      for (int k = 0; k < 4; ++k) ov[k] = ldx(rs, oO + k * 1024, sb);
#pragma unroll
      for (int k = 0; k < 4; ++k) { a0 = mfma16(W2[2 * k], asfrag(ov[k]), k ? a0 : z4); a1 = mfma16(W2[2 * k + 1], asfrag(ov[k]), k ? a1 : z4); }
      u32 mxv = 0;
#pragma unroll
      for (int k = 0; k < 4; ++k) mxv = max(max(mxv, max(ov[k].x, ov[k].y)), max(ov[k].z, ov[k].w));
      if (!__all(mxv != SENT)) {
        ++ns3; d3 = min(d3 + CH_INC, CH_D3 + 10);
        POLL(4, ov, ldx(rs, oO + k * 1024, sb), ns3);
#pragma unroll
        for (int k = 0; k < 4; ++k) { a0 = mfma16(W2[2 * k], asfrag(ov[k]), k ? a0 : z4); a1 = mfma16(W2[2 * k + 1], asfrag(ov[k]), k ? a1 : z4); }
      }
    }
    TS(9);
    {
      f32x4 a;
#pragma unroll
      for (int e = 0; e < 4; ++e) a[e] = a0[e] + ror8(a1[e]);
      if (g2 == 0) *(f32x4*)(R2 + w * 160 + r * 20 + 4 * q) = a;
    }
    asm volatile("s_waitcnt vmcnt(0)" ::: "memory");
    BARL();
    if (tid == 192) ctl_add(ctl + C_KM + s * 32 + h, 1);
    TS(10);
    if (w == 1) {
      const int n2 = l & 7, rr = l >> 3;
      f32x2 rr8[8];
#pragma unroll
      for (int ww = 0; ww < 8; ++ww) rr8[ww] = *(const f32x2*)(R2 + ww * 160 + rr * 20 + 2 * n2);
      __builtin_amdgcn_sched_barrier(0);
      const f32x2 sum = ((rr8[0] + rr8[1]) + (rr8[2] + rr8[3])) + ((rr8[4] + rr8[5]) + (rr8[6] + rr8[7]));
      const u32 v = nos(cvt_pk(sum[0], sum[1]));
      const u32x4 o = {(u32)__builtin_amdgcn_update_dpp(0, (int)v, 0x00, 0xF, 0xF, true), (u32)__builtin_amdgcn_update_dpp(0, (int)v, 0x55, 0xF, 0xF, true),
                       (u32)__builtin_amdgcn_update_dpp(0, (int)v, 0xAA, 0xF, 0xF, true), (u32)__builtin_amdgcn_update_dpp(0, (int)v, 0xFF, 0xF, 0xF, true)};
      if ((l & 3) == 0) stx(rs, pM, (u32)((s + 1) & 3) * (2 * SLOT), o);
    }
  }
}
constexpr int G_HT = 128 * 64;
DEV int lds_byte(int r, int c) { int st = (r >> 4) * 2 + (c >> 5), rr = r & 15, cc = c & 31, ob = rr * 64 + cc * 2; return st * 1024 + (ob ^ (((ob >> 9) & 1) << 5)); }
DEV void stage_rc(int b, int& R, int& C) { int st = b / 1024, sb = b % 1024, swz = sb ^ (((sb >> 9) & 1) << 5); R = (st >> 1) * 16 + swz / 64; C = (st & 1) * 32 + (swz % 64) / 2; }
DEV int swzb(int row) { return ((row & 3) << 2) | ((row >> 2) & 3); }
DEV float mish(float h) {
  const float u = __expf(-fabsf(h));
  const float n = h >= 0.f ? 1.f + 2.f * u : u * (u + 2.f);
  const float d = h >= 0.f ? n + 2.f * u * u : n + 2.f;
  return h * n * __builtin_amdgcn_rcpf(d);
}
DEV void gemm256(const int wv, const int MODE, const u16* __restrict__ A, const int lda, const u16* __restrict__ B, const int ldb, const int K, void* C, const int ldc, const u16* bias, char* smem) {
  u16* shm = (u16*)smem;
#define SA(b, h) (shm + ((b) * 2 + (h)) * G_HT)
#define SB(b, h) (shm + (4 + (b) * 2 + (h)) * G_HT)
#define STA_X(T, VO, P, br, kt) do { const int _s = ((br) * lda + (kt) * 64) * 2; \
    __builtin_amdgcn_raw_ptr_buffer_load_lds(rsa, (LDSP void*)((char*)(P) + (T) * 16), 16, (VO), _s, 0, 0); \
    __builtin_amdgcn_raw_ptr_buffer_load_lds(rsa, (LDSP void*)((char*)(P) + (T) * 16 + 8192), 16, (VO), _s + 128 * lda, 0, 0); } while (0)
#define STB_X(T, VO, P, bc, kt) do { const int _s = ((kt) * 64 * ldb + (bc)) * 2; \
    __builtin_amdgcn_raw_ptr_buffer_load_lds(rsb, (LDSP void*)((char*)(P) + (T) * 16), 16, (VO), _s, 0, 0); \
    __builtin_amdgcn_raw_ptr_buffer_load_lds(rsb, (LDSP void*)((char*)(P) + (T) * 16 + 8192), 16, (VO), _s + 64 * ldb, 0, 0); } while (0)
#define STA(P, br, kt) STA_X(t0, voa, P, br, kt)
#define STB(P, bc, kt) STB_X(t0, vob, P, bc, kt)
#define LDA(dst, b, h) for (int m = 0; m < 4; ++m) for (int k = 0; k < 2; ++k) dst[m][k] = *reinterpret_cast<const bf16x8*>((char*)SA(b, h) + lds_byte(wr * 64 + m * 16 + fr, k * 32 + fq * 8))
#define LDB(dst, b, h) for (int n = 0; n < 2; ++n) for (int k = 0; k < 2; ++k) { const char* _p = (const char*)SB(b, h) + k * 8192; \
      dst[n][k] = cat4(trread(_p + bo[n][0]), trread(_p + bo[n][1])); }
#define MMA(ai, bj, At, Bt) do { __builtin_amdgcn_s_setprio(1); \
    for (int m = 0; m < 4; ++m) for (int n = 0; n < 2; ++n) for (int k = 0; k < 2; ++k) acc[ai][bj][m][n] = mfma16(Bt[n][k], At[m][k], acc[ai][bj][m][n]); \
    __builtin_amdgcn_s_setprio(0); } while (0)
#define WAIT_V(n) asm volatile("s_waitcnt vmcnt(" #n ")" ::: "memory")
#define WAIT_L(n) asm volatile("s_waitcnt lgkmcnt(" #n ")" ::: "memory")
#define BAR __builtin_amdgcn_s_barrier()
#define SCHED __builtin_amdgcn_sched_barrier(0)
  const int t0 = tid_of(wv);
  int wid = t0 >> 6, lane = t0 & 63, wr = wid >> 2, wc = wid & 3, fr = lane & 15, fq = lane >> 4;
  f32x4 acc[2][2][4][2] = {};
  bf16x8 At[4][2], B0[2][2], B1[2][2];
  int bo[2][2];
  for (int n = 0; n < 2; ++n) for (int t = 0; t < 2; ++t) { const int row = 8 * fq + 4 * t + (fr >> 2), ch = 4 * wc + (fr & 3); bo[n][t] = 256 * row + 16 * (ch ^ swzb(row)) + 8 * n; }
  const int nt = K / 64;
  const __amdgpu_buffer_rsrc_t rsa = __builtin_amdgcn_make_buffer_rsrc((void*)uni(A), 0, 0x7FFFFFFF, 0x00020000), rsb = __builtin_amdgcn_make_buffer_rsrc((void*)uni(B), 0, 0x7FFFFFFF, 0x00020000);
  u32 voa, vob;
  { int _r, _c; stage_rc(t0 * 16, _r, _c); voa = (u32)(_r * lda + _c) * 2u; const int _b = t0 * 16, _rb = _b >> 8; vob = (u32)(_rb * ldb + ((((_b >> 4) & 15) ^ swzb(_rb)) * 8)) * 2u; }
  STB(SB(0, 0), 0, 0); STA(SA(0, 0), 0, 0); STB(SB(0, 1), 128, 0); STA(SA(0, 1), 128, 0);
  if (wr == 1) BAR;
  WAIT_V(4); BAR;
  STB(SB(1, 0), 0, 1); STA(SA(1, 0), 0, 1); STB(SB(1, 1), 128, 1);
  WAIT_V(6); BAR;
  for (int t = 0; t < nt - 2; t += 2) {
    LDB(B0, 0, 0); SCHED; LDA(At, 0, 0); STA(SA(1, 1), 128, t + 1);
    WAIT_L(8); BAR; WAIT_L(0); MMA(0, 0, At, B0); BAR; SCHED;
    LDB(B1, 0, 1); STB(SB(0, 0), 0, t + 2);
    BAR; WAIT_L(0); MMA(0, 1, At, B1); BAR;
    LDA(At, 0, 1); STA(SA(0, 0), 0, t + 2);
    BAR; WAIT_L(0); MMA(1, 0, At, B0); BAR; SCHED;
    STB(SB(0, 1), 128, t + 2);
    WAIT_V(6); BAR; MMA(1, 1, At, B1); BAR;
    LDB(B0, 1, 0); SCHED; LDA(At, 1, 0); STA(SA(0, 1), 128, t + 2);
    WAIT_L(8); BAR; WAIT_L(0); MMA(0, 0, At, B0); BAR; SCHED;
    LDB(B1, 1, 1); STB(SB(1, 0), 0, t + 3);
    BAR; WAIT_L(0); MMA(0, 1, At, B1); BAR;
    LDA(At, 1, 1); STA(SA(1, 0), 0, t + 3);
    BAR; WAIT_L(0); MMA(1, 0, At, B0); BAR; SCHED;
    STB(SB(1, 1), 128, t + 3);
    WAIT_V(6); BAR; MMA(1, 1, At, B1); BAR;
  }
  int tz = tid_of(wv); wid = tz >> 6; lane = tz & 63; wr = wid >> 2; wc = wid & 3; fr = lane & 15; fq = lane >> 4;
  { LDB(B0, 0, 0); WAIT_V(0); LDA(At, 0, 0); { int _r, _c; stage_rc(tz * 16, _r, _c); const u32 voa2 = (u32)(_r * lda + _c) * 2u; STA_X(tz, voa2, SA(1, 1), 128, nt - 1); }
    BAR; WAIT_L(0); MMA(0, 0, At, B0); BAR;
    LDB(B1, 0, 1); BAR; WAIT_L(0); MMA(0, 1, At, B1); BAR;
    LDA(At, 0, 1); WAIT_V(4); BAR; WAIT_L(0); MMA(1, 0, At, B0); MMA(1, 1, At, B1); BAR; }
  { LDB(B0, 1, 0); LDA(At, 1, 0); WAIT_V(2); BAR; WAIT_L(0); MMA(0, 0, At, B0); BAR;
    LDB(B1, 1, 1); WAIT_V(0); BAR; WAIT_L(0); MMA(0, 1, At, B1); BAR;
    LDA(At, 1, 1); BAR; WAIT_L(0); MMA(1, 0, At, B0); MMA(1, 1, At, B1); BAR; }
  if (wr == 0) BAR;
  const __amdgpu_buffer_rsrc_t rc = __builtin_amdgcn_make_buffer_rsrc((void*)uni((char*)C), 0, 0x7FFFFFFF, 0x00020000);
#pragma unroll
  for (int ai = 0; ai < 2; ++ai)
#pragma unroll
  for (int bj = 0; bj < 2; ++bj)
#pragma unroll
  for (int m = 0; m < 4; ++m) {
    const int row = ai * 128 + wr * 64 + m * 16 + fr, col = bj * 128 + wc * 32 + fq * 8;
    f32x4 v0 = acc[ai][bj][m][0], v1 = acc[ai][bj][m][1];
    if (MODE == 4) {
      const __amdgpu_buffer_rsrc_t ry = __builtin_amdgcn_make_buffer_rsrc((void*)bias, 0, 0x7FFFFFFF, 0x00020000);
      const u32 o = (u32)(row * ldc + col) * 4;
      v0 += __builtin_bit_cast(f32x4, __builtin_amdgcn_raw_buffer_load_b128(ry, o, 0, 16)) + __builtin_bit_cast(f32x4, __builtin_amdgcn_raw_buffer_load_b128(ry, o, 8u << 20, 16));
      v1 += __builtin_bit_cast(f32x4, __builtin_amdgcn_raw_buffer_load_b128(ry, o + 16, 0, 16)) + __builtin_bit_cast(f32x4, __builtin_amdgcn_raw_buffer_load_b128(ry, o + 16, 8u << 20, 16));
      __builtin_amdgcn_raw_buffer_store_b128(u32x4{cvt_pk(v0[0], v0[1]), cvt_pk(v0[2], v0[3]), cvt_pk(v1[0], v1[1]), cvt_pk(v1[2], v1[3])}, rc, (row * ldc + col) * 2, 0, 0);
    } else if (MODE >= 2) {
      if (MODE == 2) { const u32x4 xv = *(const u32x4*)(bias + (long)row * ldc + col);
        v0[0] += bf_lo(xv.x); v0[1] += bf_hi(xv.x); v0[2] += bf_lo(xv.y); v0[3] += bf_hi(xv.y); v1[0] += bf_lo(xv.z); v1[1] += bf_hi(xv.z); v1[2] += bf_lo(xv.w); v1[3] += bf_hi(xv.w); }
      __builtin_amdgcn_raw_buffer_store_b128(__builtin_bit_cast(u32x4, v0), rc, (row * ldc + col) * 4, 0, 16);
      __builtin_amdgcn_raw_buffer_store_b128(__builtin_bit_cast(u32x4, v1), rc, (row * ldc + col) * 4 + 16, 0, 16);
    } else {
      if (MODE == 1) { const u32x4 bb = *(const u32x4*)(bias + col);
        v0[0] = mish(v0[0] + bf_lo(bb.x)); v0[1] = mish(v0[1] + bf_hi(bb.x)); v0[2] = mish(v0[2] + bf_lo(bb.y)); v0[3] = mish(v0[3] + bf_hi(bb.y));
        v1[0] = mish(v1[0] + bf_lo(bb.z)); v1[1] = mish(v1[1] + bf_hi(bb.z)); v1[2] = mish(v1[2] + bf_lo(bb.w)); v1[3] = mish(v1[3] + bf_hi(bb.w)); }
      __builtin_amdgcn_raw_buffer_store_b128(u32x4{cvt_pk(v0[0], v0[1]), cvt_pk(v0[2], v0[3]), cvt_pk(v1[0], v1[1]), cvt_pk(v1[2], v1[3])}, rc, (row * ldc + col) * 2, 0, 16);
    }
  }
#undef SA
#undef SB
#undef LDA
#undef LDB
#undef MMA
}
DEV void wait_cnts(const int wv, u32* ctl, int ia, int na, u32 ta, int ib, int nb, u32 tb);
DEV void attn_item(const int wv, const Params& p, int s, int h, char* lds) {
  const int t0 = tid_of(0);
  const int l = t0 & 63, q = l >> 4, i = l & 15;
  const __amdgpu_buffer_rsrc_t rq = __builtin_amdgcn_make_buffer_rsrc((void*)((const u16*)(p.ws + WS_Q) + (size_t)s * 32 * HD + h * 128), 0, 0x7FFFFFFF, 0x00020000);
  const __amdgpu_buffer_rsrc_t rm = __builtin_amdgcn_make_buffer_rsrc((void*)((const u16*)(p.ws + WS_KM) + (size_t)s * 8 * HD + h * 128), 0, 0x7FFFFFFF, 0x00020000);
  const u16* K = (const u16*)(p.ws + WS_K) + (size_t)s * 32 * HD + h * 128;
  const u16* V = (const u16*)(p.ws + WS_V) + (size_t)s * 32 * HD + h * 128;
  u32x4 vv[8]; bf16x8 qf[4][2], kf[4][2];
#pragma unroll
  for (int it = 0; it < 8; ++it) vv[it] = *(const u32x4*)(V + (size_t)(it * 4 + q) * HD + i * 8);
#pragma unroll
  for (int ks = 0; ks < 4; ++ks) {
    const int e = ks * 32 + q * 8;
    qf[ks][0] = asfrag(__builtin_amdgcn_raw_buffer_load_b128(rq, (i * HD + e) * 2, 0, 16)); qf[ks][1] = asfrag(__builtin_amdgcn_raw_buffer_load_b128(rq, ((16 + i) * HD + e) * 2, 0, 16));
    kf[ks][0] = *(const bf16x8*)(K + (size_t)i * HD + e); kf[ks][1] = *(const bf16x8*)(K + (size_t)(16 + i) * HD + e);
  }
  __builtin_amdgcn_sched_barrier(0);
#pragma unroll
  for (int it = 0; it < 8; ++it) { const int row = 16 + it * 4 + q; *(u32x4*)(lds + row * 256 + 16 * (i ^ swzb(row))) = vv[it]; }
#pragma unroll
  for (int it = 2; it < 4; ++it) { const int row = it * 4 + q; *(u32x4*)(lds + row * 256 + 16 * (i ^ swzb(row))) = u32x4{0, 0, 0, 0}; }
  f32x4 c[3][2];
#pragma unroll
  for (int t = 0; t < 3; ++t) c[t][0] = c[t][1] = f32x4{0, 0, 0, 0};
#pragma unroll
  for (int ks = 0; ks < 4; ++ks) { c[1][0] = mfma16(kf[ks][0], qf[ks][0], c[1][0]); c[1][1] = mfma16(kf[ks][0], qf[ks][1], c[1][1]); c[2][1] = mfma16(kf[ks][1], qf[ks][1], c[2][1]); }
  wait_cnts(wv, (u32*)(p.ws + WS_CTL), C_KM + s * 32 + (h - wv), 8, 4, 0, 0, 0);
  __syncthreads();
  {
    u32x4 vm[2]; bf16x8 km[4];
#pragma unroll
    for (int it = 0; it < 2; ++it) vm[it] = __builtin_amdgcn_raw_buffer_load_b128(rm, (4u << 20) + ((it * 4 + q) * HD + i * 8) * 2, 0, 16);
#pragma unroll
    for (int ks = 0; ks < 4; ++ks) km[ks] = asfrag(__builtin_amdgcn_raw_buffer_load_b128(rm, ((i & 7) * HD + ks * 32 + q * 8) * 2, 0, 16));
#pragma unroll
    for (int it = 0; it < 2; ++it) { const int row = it * 4 + q; *(u32x4*)(lds + row * 256 + 16 * (i ^ swzb(row))) = vm[it]; }
#pragma unroll
    for (int ks = 0; ks < 4; ++ks) { c[0][0] = mfma16(km[ks], qf[ks][0], c[0][0]); c[0][1] = mfma16(km[ks], qf[ks][1], c[0][1]); }
  }
  const float sc = 0.08838834764831845f * 1.4426950408889634f, NEG = -3.0e38f;
  u32x4 pt[2], pm[2];
#pragma unroll
  for (int u = 0; u < 2; ++u) {
    float sm[4], s1[4], s2[4]; float mx = NEG;
#pragma unroll
    for (int e = 0; e < 4; ++e) {
      sm[e] = q < 2 ? c[0][u][e] * sc : NEG;
      s1[e] = (u == 1 || 4 * q + e <= i) ? c[1][u][e] * sc : NEG;
      s2[e] = (u == 1 && 4 * q + e <= i) ? c[2][u][e] * sc : NEG;
      mx = fmaxf(mx, fmaxf(sm[e], fmaxf(s1[e], s2[e])));
    }
    mx = red32_max(red16_max(mx));
    float sum = 0;
#pragma unroll
    for (int e = 0; e < 4; ++e) {
      sm[e] = q < 2 ? __builtin_amdgcn_exp2f(sm[e] - mx) : 0.f;
      s1[e] = (u == 1 || 4 * q + e <= i) ? __builtin_amdgcn_exp2f(s1[e] - mx) : 0.f;
      s2[e] = (u == 1 && 4 * q + e <= i) ? __builtin_amdgcn_exp2f(s2[e] - mx) : 0.f;
      sum += sm[e] + s1[e] + s2[e];
    }
    sum = red32_add(red16_add(sum));
    const float inv = 1.f / sum;
    pt[u] = u32x4{cvt_pk(s1[0] * inv, s1[1] * inv), cvt_pk(s1[2] * inv, s1[3] * inv), cvt_pk(s2[0] * inv, s2[1] * inv), cvt_pk(s2[2] * inv, s2[3] * inv)};
    pm[u] = u32x4{cvt_pk(sm[0] * inv, sm[1] * inv), cvt_pk(sm[2] * inv, sm[3] * inv), 0u, 0u};
  }
  asm volatile("s_waitcnt lgkmcnt(0)" ::: "memory");
  u16* O = (u16*)(p.ws + WS_O) + (size_t)s * 32 * HD + h * 128;
  const __amdgpu_buffer_rsrc_t ro = __builtin_amdgcn_make_buffer_rsrc((void*)O, 0, 0x7FFFFFFF, 0x00020000);
  const s16x4 zz = {0, 0, 0, 0};
#pragma unroll
  for (int cp = 0; cp < 4; ++cp) {
    f32x4 o[2][2];
#pragma unroll
    for (int c2 = 0; c2 < 2; ++c2) {
      const int ch = 4 * cp + (i & 3);
      const int rm = 4 * q + (i >> 2), r1 = 16 + rm, r2 = 32 + rm;
      const bf16x8 vm = cat4(trread(lds + rm * 256 + 16 * (ch ^ swzb(rm)) + 8 * c2), zz);
      const bf16x8 vt = cat4(trread(lds + r1 * 256 + 16 * (ch ^ swzb(r1)) + 8 * c2), trread(lds + r2 * 256 + 16 * (ch ^ swzb(r2)) + 8 * c2));
#pragma unroll
      for (int u = 0; u < 2; ++u) o[c2][u] = mfma16(vm, asfrag(pm[u]), mfma16(vt, asfrag(pt[u]), f32x4{0, 0, 0, 0}));
    }
#pragma unroll
    for (int u = 0; u < 2; ++u)
      __builtin_amdgcn_raw_buffer_store_b128(u32x4{cvt_pk(o[0][u][0], o[0][u][1]), cvt_pk(o[0][u][2], o[0][u][3]), cvt_pk(o[1][u][0], o[1][u][1]), cvt_pk(o[1][u][2], o[1][u][3])}, ro, ((16 * u + i) * HD + 32 * cp + 8 * q) * 2, 0, 16);
  }
}

template <int BM, int LDA, int LDB, int AUXA>
DEV void small_gemm(const int wv, const u16* A, const u16* B, f32x4 (&acc)[BM / 32][2], char* smem) {
  constexpr int MT = BM / 32, NLD = BM / 64 + 2, NS = (BM == 128) ? 4 : 5;
  const int tid = tid_of(wv);
  const int lane = tid & 63, wr = wv >> 2, wc = wv & 3, fr = lane & 15, fq = lane >> 4;
  const __amdgpu_buffer_rsrc_t rsa = __builtin_amdgcn_make_buffer_rsrc((void*)uni(A), 0, 0x7FFFFFFF, 0x00020000), rsb = __builtin_amdgcn_make_buffer_rsrc((void*)uni(B), 0, 0x7FFFFFFF, 0x00020000);
  u32 voa, vob;
  { int r, c; stage_rc(tid * 16, r, c); voa = (u32)(r * LDA + c) * 2u; const int b = tid * 16, rb = b >> 8; vob = (u32)(rb * LDB + ((((b >> 4) & 15) ^ swzb(rb)) * 8)) * 2u; }
  constexpr int STG = BM * 128 + 16384;
  auto stage = [&](int kt, int slot) {
    char* sa = smem + slot * STG + tid * 16;
    __builtin_amdgcn_raw_ptr_buffer_load_lds(rsa, (LDSP void*)sa, 16, voa, kt * 128, 0, AUXA);
    if (BM == 128) __builtin_amdgcn_raw_ptr_buffer_load_lds(rsa, (LDSP void*)(sa + 8192), 16, voa, kt * 128 + 64 * LDA * 2, 0, AUXA);
    __builtin_amdgcn_raw_ptr_buffer_load_lds(rsb, (LDSP void*)(sa + BM * 128), 16, vob, kt * 64 * LDB * 2, 0, 0);
    __builtin_amdgcn_raw_ptr_buffer_load_lds(rsb, (LDSP void*)(sa + BM * 128 + 8192), 16, vob, (kt * 64 + 32) * LDB * 2, 0, 0);
  };
  int bo[2];
#pragma unroll
  for (int t = 0; t < 2; ++t) { const int row = 8 * fq + 4 * t + (fr >> 2); bo[t] = BM * 128 + 256 * row + 16 * ((4 * wc + (fr & 3)) ^ swzb(row)); }
  const int ao = lds_byte(wr * (BM / 2) + fr, fq * 8);
#pragma unroll
  for (int m = 0; m < MT; ++m) acc[m][0] = acc[m][1] = f32x4{0, 0, 0, 0};
#pragma unroll
  for (int k = 0; k < NS - 1; ++k) stage(k, k);
  constexpr int nt = 16;
  int cur = 0, nxt = NS - 1;
  for (int kt = 0; kt < nt; ++kt) {
    if (kt + NS - 2 < nt) asm volatile("s_waitcnt vmcnt(%0)" ::"n"((NS - 2) * NLD) : "memory"); else asm volatile("s_waitcnt vmcnt(0)" ::: "memory");
    __builtin_amdgcn_s_barrier();
    if (kt + NS - 1 < nt) stage(kt + NS - 1, nxt);
    const char* sa = smem + cur * STG;
#pragma unroll
    for (int k = 0; k < 2; ++k) {
      const bf16x8 b0 = cat4(trread(sa + k * 8192 + bo[0]), trread(sa + k * 8192 + bo[1])), b1 = cat4(trread(sa + k * 8192 + bo[0] + 8), trread(sa + k * 8192 + bo[1] + 8));
#pragma unroll
      for (int m = 0; m < MT; ++m) {
        const bf16x8 a = *(const bf16x8*)(sa + ao + m * 2048 + k * 1024);
        acc[m][0] = mfma16(b0, a, acc[m][0]); acc[m][1] = mfma16(b1, a, acc[m][1]);
      }
    }
    asm volatile("s_waitcnt lgkmcnt(0)" ::: "memory");
    cur = cur == NS - 1 ? 0 : cur + 1; nxt = nxt == NS - 1 ? 0 : nxt + 1;
  }
}
template <int BM>
DEV void wo_item(const int wv, const Params& p, int g, int nt_, int kq, char* smem) {
  constexpr int MT = BM / 32;
  f32x4 acc[MT][2];
  small_gemm<BM, HD, D_, 16>(wv, (const u16*)(p.ws + WS_O) + (size_t)g * BM * HD + kq * 1024, p.Wo + (size_t)kq * 1024 * D_ + nt_ * 128, acc, smem);
  const int tid = tid_of(wv);
  const int lane = tid & 63, wr = wv >> 2, wc = wv & 3, fr = lane & 15, fq = lane >> 4;
  const int tile = (BM == 128 ? g : g - 15) * 8 + nt_;
  char* part = p.ws + WS_P + (size_t)tile * (4 * 65536);
  const __amdgpu_buffer_rsrc_t rp = __builtin_amdgcn_make_buffer_rsrc((void*)part, 0, 0x7FFFFFFF, 0x00020000);
#pragma unroll
  for (int m = 0; m < MT; ++m)
#pragma unroll
    for (int n = 0; n < 2; ++n) __builtin_amdgcn_raw_buffer_store_b128(__builtin_bit_cast(u32x4, acc[m][n]), rp, tid * 16 + (m * 2 + n) * 8192, kq * 65536, 16);
  asm volatile("s_waitcnt vmcnt(0)" ::: "memory");
  __syncthreads();
  volatile int* s_last = (volatile int*)(smem + 128 * 1024 + 32);
  if (tid == 0) *s_last = (int)ctl_add((u32*)(p.ws + WS_CTL) + C_P + tile, 1);
  __syncthreads();
  if (__builtin_amdgcn_readfirstlane(*s_last) != 3) return;
  if (wv == 0) {
    u32* cy = (u32*)(p.ws + WS_CTL) + C_Y + (g * BM >> 8) * 4 + (nt_ >> 1);
    for (int sp = 0; __builtin_amdgcn_readfirstlane(ctl_ld(cy)) < 2; ++sp) { __builtin_amdgcn_s_sleep(6); if (sp > 400000) { ctl_st((u32*)(p.ws + WS_CTL) + C_TMO, 1); break; } }
  }
  __syncthreads();
  const float* Y1 = (const float*)(p.ws + WS_Y); const float* Y2 = (const float*)(p.ws + WS_Y + (8u << 20));
  const __amdgpu_buffer_rsrc_t ry = __builtin_amdgcn_make_buffer_rsrc((void*)Y1, 0, 0x7FFFFFFF, 0x00020000);
#pragma unroll
  for (int m = 0; m < MT; ++m) {
    const u32 o = (u32)(g * BM + wr * (BM / 2) + m * 16 + fr) * D_ + nt_ * 128 + wc * 32 + fq * 8;
    f32x4 v[2];
#pragma unroll
    for (int n = 0; n < 2; ++n) {
      f32x4 t[4];
#pragma unroll
      for (int k = 0; k < 4; ++k) t[k] = __builtin_bit_cast(f32x4, __builtin_amdgcn_raw_buffer_load_b128(rp, tid * 16 + (m * 2 + n) * 8192, k * 65536, 16));
      const f32x4 y1 = __builtin_bit_cast(f32x4, __builtin_amdgcn_raw_buffer_load_b128(ry, o * 4 + n * 16, 0, 16)), y2 = __builtin_bit_cast(f32x4, __builtin_amdgcn_raw_buffer_load_b128(ry, o * 4 + n * 16, 8u << 20, 16));
      v[n] = ((t[0] + t[1]) + (t[2] + t[3])) + (y1 + y2);
    }
    *(u32x4*)(p.out + o) = u32x4{cvt_pk(v[0][0], v[0][1]), cvt_pk(v[0][2], v[0][3]), cvt_pk(v[1][0], v[1][1]), cvt_pk(v[1][2], v[1][3])};
  }
}

DEV void kv0_item(const int wv, const Params& p, int j, char* smem) {
  const int hf = j & 1, mat = (j >> 1) & 1, n0 = (j >> 2) * 128;
  f32x4 acc[4][2];
  small_gemm<128, D_, HD, 0>(wv, (const u16*)(p.ws + WS_XN) + (size_t)hf * 128 * D_, (mat ? p.Wv : p.Wk) + n0, acc, smem);
  const int tid = tid_of(wv), lane = tid & 63, wr = wv >> 2, wc = wv & 3, fr = lane & 15, fq = lane >> 4;
  const __amdgpu_buffer_rsrc_t rc = __builtin_amdgcn_make_buffer_rsrc((void*)((u16*)(p.ws + (mat ? WS_V : WS_K)) + (size_t)hf * 128 * HD + n0), 0, 0x7FFFFFFF, 0x00020000);
#pragma unroll
  for (int m = 0; m < 4; ++m)
    __builtin_amdgcn_raw_buffer_store_b128(u32x4{cvt_pk(acc[m][0][0], acc[m][0][1]), cvt_pk(acc[m][0][2], acc[m][0][3]), cvt_pk(acc[m][1][0], acc[m][1][1]), cvt_pk(acc[m][1][2], acc[m][1][3])},
                                           rc, ((wr * 64 + m * 16 + fr) * HD + wc * 32 + fq * 8) * 2, 0, 16);
}
constexpr int LDS_BYTES = 128 * 1024 + 64;
DEV void wait_cnts(const int wv, u32* ctl, int ia, int na, u32 ta, int ib, int nb, u32 tb) {
  if (wv == 0) {
    const int t = tid_of(0);
    u32* c = ctl + (t < na ? ia + t : (t < na + nb ? ib + t - na : ia));
    const u32 target = t < na ? ta : (t < na + nb ? tb : 0u);
    for (int sp = 0;; ++sp) {
      if (__all(ctl_ld(c) >= target)) break;
      __builtin_amdgcn_s_sleep(6);
      if (sp > 400000 || ((sp & 63) == 63 && __builtin_amdgcn_readfirstlane(ctl_ld(ctl + C_TMO)))) { ctl_st(ctl + C_TMO, 1); break; }
    }
  }
}
DEV void acquire_wg(const int wv) {
  if (tid_of(wv) == 0) { __builtin_amdgcn_fence(__ATOMIC_ACQUIRE, "agent"); asm volatile("s_waitcnt vmcnt(0)" ::: "memory"); }
  __syncthreads();
}
DEV void signal_wg(const int wv, u32* c) {
  asm volatile("s_waitcnt vmcnt(0)" ::: "memory");
  __syncthreads();
  if (tid_of(wv) == 0) ctl_add(c, 1);
}
#define NBIG 0
constexpr int N_KV0 = 128, N_KV = N_KV0 + 224, N_F1 = 112, N_Q = 128, N_Y = 64, N_G = 16 * 48, I_F1 = N_KV, I_Q = I_F1 + N_F1, I_Y = I_Q + N_Q, I_G = I_Y + N_Y, N_ITEMS = I_G + N_G;
typedef __attribute__((address_space(4))) const Params* KArg;
DEV void worker(const int wv, const int role, char* smem) {
  volatile int* s_idx = (volatile int*)(smem + 128 * 1024 + 16);
  int qn = 4, tried = 0;
  for (;;) {
    KArg ka = (KArg)__builtin_amdgcn_kernarg_segment_ptr(); asm volatile("" : "+s"(ka));
    Params p; p.x = ka->x; p.mem0 = ka->mem0; p.Wq = ka->Wq; p.Wk = ka->Wk; p.Wv = ka->Wv; p.Wo = ka->Wo; p.w1 = ka->w1; p.b1 = ka->b1; p.w2 = ka->w2; p.out = ka->out; p.ws = ka->ws;
    u32* ctl = (u32*)(p.ws + WS_CTL);
    const u16* XN = (const u16*)(p.ws + WS_XN);
    __syncthreads();
    if (tid_of(wv) == 0) *s_idx = (int)ctl_add(ctl + C_QUEUE + qn * 32, 1);
    __syncthreads();
    int idx = __builtin_amdgcn_readfirstlane(*s_idx);
    if (qn == 4) { if (idx >= I_G) { qn = (role >> 5) & 3; continue; } }
    else {
      if (idx >= 200) { if (++tried == 4) break; qn = (qn + 1) & 3; continue; }
      int g, k;
      if (idx < 4) { g = 0; k = idx; } else { const int u = idx - 4, gg = u / 12, kk = u - gg * 12;
        if (gg < 15) { if (kk < 4) { g = gg + 1; k = kk; } else { g = gg; k = kk; } } else { g = 15; k = u - 180 + 4; } }
      if (g < 2 * NBIG && k >= 4) {
        if (!(g & 1) || k != 4) continue;
        idx = I_G + N_G + 64 + (g >> 1) * 4 + qn;
      } else
      idx = (g == 15 && k >= 4) ? I_G + N_G + qn * 16 + (k - 4) : I_G + g * 48 + (k < 4 ? k * 4 + qn : 16 + qn * 8 + (k - 4));
    }
    if (idx < N_KV0) {
      kv0_item(wv, p, idx, smem);
      signal_wg(wv, ctl + C_KV);
    } else if (idx < I_G || idx >= I_G + N_G + 64) {
      const u16 *A, *B, *bias; void* C; int lda, ldb, K, ldc, mode; u32* done;
      if (idx < I_F1) { const int jj = idx - N_KV0 + 32, mt = jj >> 5, mat = (jj >> 4) & 1, n0 = (jj & 15) * 256;
        A = XN + (size_t)mt * 256 * D_; lda = D_; B = (mat ? p.Wv : p.Wk) + n0; ldb = HD; K = D_; C = (u16*)(p.ws + (mat ? WS_V : WS_K)) + (size_t)mt * 256 * HD + n0; ldc = HD; bias = p.b1; mode = 0; done = ctl + C_KV + mt * 32;
      } else if (idx < I_Q) { const int j = idx - I_F1, mt = j & 7, n0 = (j >> 3) * 256;
        A = XN + (size_t)mt * 256 * D_; lda = D_; B = p.w1 + n0; ldb = FF; K = D_; C = (u16*)(p.ws + WS_ACT) + (size_t)mt * 256 * FF + n0; ldc = FF; bias = p.b1 + n0; mode = 1; done = ctl + C_F1 + mt * 32;
      } else if (idx < I_Y) { const int j = idx - I_Q, mt = j >> 4, n0 = (j & 15) * 256;
        A = XN + (size_t)mt * 256 * D_; lda = D_; B = p.Wq + n0; ldb = HD; K = D_; C = (u16*)(p.ws + WS_Q) + (size_t)mt * 256 * HD + n0; ldc = HD; bias = p.b1; mode = 0; done = ctl + C_Q + j;
      } else if (idx >= I_G) { const int j = idx - (I_G + N_G + 64), mt = j >> 2, n0 = (j & 3) * 256;
        wait_cnts(wv, ctl, C_A + mt * 8, 8, 4, C_Y + mt * 4 + (j & 3), 1, 2); acquire_wg(wv);
        A = (const u16*)(p.ws + WS_O) + (size_t)mt * 256 * HD; lda = HD; B = p.Wo + n0; ldb = D_; K = HD; C = p.out + (size_t)mt * 256 * D_ + n0; ldc = D_;
        bias = (const u16*)((const float*)(p.ws + WS_Y) + (size_t)mt * 256 * D_ + n0); mode = 4; done = ctl + C_TMO + 1;
      } else { const int j = idx - I_Y, kh = j & 1, mt = j >> 3, n0 = ((j >> 1) & 3) * 256;
        wait_cnts(wv, ctl, C_F1 + mt * 32, 1, 14, 0, 0, 0); acquire_wg(wv);
        A = (const u16*)(p.ws + WS_ACT) + (size_t)mt * 256 * FF + kh * 1792; lda = FF; B = p.w2 + (size_t)kh * 1792 * D_ + n0; ldb = D_; K = 1792;
        C = (float*)(p.ws + WS_Y + (size_t)kh * (8u << 20)) + (size_t)mt * 256 * D_ + n0; ldc = D_; bias = p.x + (size_t)mt * 256 * D_ + n0; mode = kh ? 3 : 2; done = ctl + C_Y + (j >> 1);
      }
      const int dn = __builtin_amdgcn_readfirstlane((int)(done - ctl));
      gemm256(wv, __builtin_amdgcn_readfirstlane(mode), A, __builtin_amdgcn_readfirstlane(lda), B, __builtin_amdgcn_readfirstlane(ldb), __builtin_amdgcn_readfirstlane(K), C, __builtin_amdgcn_readfirstlane(ldc), uni(bias), smem);
      KArg kb = (KArg)__builtin_amdgcn_kernarg_segment_ptr(); asm volatile("" : "+s"(kb));
      signal_wg(wv, (u32*)(kb->ws + WS_CTL) + dn);
    } else if (idx >= I_G + N_G) {
      const int j = idx - I_G - N_G, kq = j >> 4, nt_ = j & 7, hf = (j >> 3) & 1;
      wait_cnts(wv, ctl, C_A + 60 + kq, 1, 4, 0, 0, 0);
      __syncthreads();
      wo_item<64>(wv, p, 30 + hf, nt_, kq, smem);
    } else {
      const int j = idx - I_G, g = j / 48, k = j - g * 48;
      if (k < 16) {
        const int seg = 4 * g + (k >> 2), h0 = 8 * (k & 3);
        wait_cnts(wv, ctl, C_Q + (seg >> 3) * 16 + (h0 >> 1), 4, 1, C_KV + (seg >> 3) * 32, 1, seg < 8 ? 128 : 32);
        __syncthreads();
        attn_item(wv, p, seg, h0 + wv, smem + wv * 12288);
        signal_wg(wv, ctl + C_A + g * 4 + (k & 3));
      } else {
        const int kq = (k - 16) >> 3, nt_ = (k - 16) & 7;
        wait_cnts(wv, ctl, C_A + g * 4 + kq, 1, 4, 0, 0, 0);
        __syncthreads();
        wo_item<128>(wv, p, g, nt_, kq, smem);
      }
    }
  }
}
__global__ void __launch_bounds__(512, 2) k_main(Params p) {
  extern __shared__ __attribute__((aligned(16))) char smem[];
  const int wv = __builtin_amdgcn_readfirstlane(threadIdx.x >> 6);
  const int role = take_role((u32*)(p.ws + WS_CTL), (int*)(smem + 128 * 1024));
  if (role >= 0 && role < 128) chain_role(p, role, smem);
  __syncthreads();
  worker(wv, role, smem);
}
extern "C" void kernel_launch(void* const* d_in, const int* in_sizes, int n_in, void* d_out, int out_size, void* d_ws, size_t ws_size, hipStream_t stream) {
  Params p;
  p.x = (const u16*)d_in[0]; p.mem0 = (const u16*)d_in[1]; p.Wq = (const u16*)d_in[2]; p.Wk = (const u16*)d_in[3]; p.Wv = (const u16*)d_in[4]; p.Wo = (const u16*)d_in[5];
  p.w1 = (const u16*)d_in[8]; p.b1 = (const u16*)d_in[9]; p.w2 = (const u16*)d_in[10]; p.out = (u16*)d_out; p.ws = (char*)d_ws;
  (void)hipFuncSetAttribute((const void*)k_main, hipFuncAttributeMaxDynamicSharedMemorySize, LDS_BYTES);
  k_prep<<<512, 256, 0, stream>>>(p);
  k_main<<<256, 512, LDS_BYTES, stream>>>(p);
}
```

```cpp
#include <hip/hip_runtime.h>
#include <hip/hip_bf16.h>
#include <stdint.h>

typedef unsigned int u32;
typedef unsigned short u16;
typedef u32 u32x4 __attribute__((ext_vector_type(4)));
typedef u32 u32x2 __attribute__((ext_vector_type(2)));
typedef short bf16x8 __attribute__((ext_vector_type(8)));
typedef short s16x4 __attribute__((ext_vector_type(4)));
typedef float f32x4 __attribute__((ext_vector_type(4)));
typedef __bf16 bf2_t __attribute__((ext_vector_type(2)));
typedef float f32x2 __attribute__((ext_vector_type(2)));
#define LDSP __attribute__((address_space(3)))
#define DEV __device__ __forceinline__

constexpr int S_ = 2048, D_ = 1024, HD = 4096, FF = 3584, NSEG = 64;
constexpr u32 SENT = 0xFFFFFFFFu;
constexpr size_t WS_XN = 0;
constexpr size_t WS_Q = 4u << 20;
constexpr size_t WS_K = 20u << 20;
constexpr size_t WS_V = 36u << 20;
constexpr size_t WS_ACT = 52u << 20;
constexpr size_t WS_O = 68u << 20;
constexpr size_t WS_KM = 84u << 20;
constexpr size_t WS_VM = 88u << 20;
constexpr size_t WS_CTL = 92u << 20;
constexpr size_t WS_XCH = 96u << 20;
constexpr size_t WS_Y = 100u << 20;
constexpr size_t WS_P = 116u << 20;
constexpr u32 SLOT = 256 * 1024, X_M = 0, X_O = 32768, X_SC = 98304;
constexpr int C_TICK = 0;
constexpr int C_QUEUE = 1344  , C_F1 = 512, C_Q = C_F1 + 8 * 32, C_Y = C_Q + 128, C_A = C_Y + 32  , C_P = C_A + 128  , C_KV = 1600  , C_KM = 2048;
constexpr int C_OVF = 8 * 32, C_TOTAL = 9 * 32, C_NEAR = 10 * 32, C_TMO = 11 * 32;

DEV u32 cvt_pk(float a, float b) { f32x2 v = {a, b}; bf2_t r = __builtin_convertvector(v, bf2_t); return __builtin_bit_cast(u32, r); }
DEV float bf_lo(u32 x) { return __uint_as_float(x << 16); }
DEV float bf_hi(u32 x) { return __uint_as_float(x & 0xFFFF0000u); }
DEV u32 nosent(u32 x) { return x == SENT ? 0xFFFEFFFFu : x; }
DEV float ror8(float x) { return __int_as_float(__builtin_amdgcn_update_dpp(0, __float_as_int(x), 0x128, 0xF, 0xF, true)); }
DEV u32 ror8u(u32 x) { return (u32)__builtin_amdgcn_update_dpp(0, (int)x, 0x128, 0xF, 0xF, true); }
DEV float xor16(float x) { u32 a = __float_as_uint(x); auto r = __builtin_amdgcn_permlane16_swap(a, a, false, false); return __uint_as_float((threadIdx.x & 16) ? r[0] : r[1]); }
DEV float xor32(float x) { u32 a = __float_as_uint(x); auto r = __builtin_amdgcn_permlane32_swap(a, a, false, false); return __uint_as_float((threadIdx.x & 32) ? r[0] : r[1]); }
DEV f32x4 mfma16(bf16x8 a, bf16x8 b, f32x4 c) { return __builtin_amdgcn_mfma_f32_16x16x32_bf16(a, b, c, 0, 0, 0); }
DEV bf16x8 asfrag(u32x4 v) { return __builtin_bit_cast(bf16x8, v); }
DEV s16x4 trread(const char* p) { return __builtin_amdgcn_ds_read_tr16_b64_v4i16((LDSP s16x4*)p); }
DEV bf16x8 cat4(s16x4 a, s16x4 b) { return __builtin_shufflevector(a, b, 0, 1, 2, 3, 4, 5, 6, 7); }
DEV bool valid4(u32x4 v) { return (v.x != SENT) & (v.y != SENT) & (v.z != SENT) & (v.w != SENT); }
DEV u32x4 ld_sc1(__amdgpu_buffer_rsrc_t rs, u32 off) { return __builtin_amdgcn_raw_buffer_load_b128(rs, off, 0, 16); }
DEV void st_sc1(__amdgpu_buffer_rsrc_t rs, u32 off, u32x4 v) { __builtin_amdgcn_raw_buffer_store_b128(v, rs, off, 0, 16); }
DEV u32 ctl_ld(u32* p) { return __hip_atomic_load(p, __ATOMIC_RELAXED, __HIP_MEMORY_SCOPE_AGENT); }
DEV void ctl_st(u32* p, u32 v) { __hip_atomic_store(p, v, __ATOMIC_RELAXED, __HIP_MEMORY_SCOPE_AGENT); }
DEV u32 ctl_add(u32* p, u32 v) { return __hip_atomic_fetch_add(p, v, __ATOMIC_RELAXED, __HIP_MEMORY_SCOPE_AGENT); }
DEV char* xch_base(char* ws) { return (char*)(((size_t)(ws + WS_XCH) + (2u << 20) - 1) & ~(size_t)((2u << 20) - 1)); }

DEV int tid_of(int wv) { int ln; asm volatile("v_mbcnt_lo_u32_b32 %0, -1, 0\n v_mbcnt_hi_u32_b32 %0, -1, %0" : "=v"(ln)); return wv * 64 + ln; }
template <class T> DEV T* uni(T* p) {
  const unsigned long long a = (unsigned long long)p;
  return (T*)(((unsigned long long)(u32)__builtin_amdgcn_readfirstlane((int)(a >> 32)) << 32) | (u32)__builtin_amdgcn_readfirstlane((int)a));
}

struct Params {
  const u16* x; const u16* mem0; const u16* Wq; const u16* Wk; const u16* Wv; const u16* Wo;
  const u16* w1; const u16* b1; const u16* w2; u16* out; char* ws;
};

__global__ void __launch_bounds__(256) k_prep(Params p) {
  const int tid = threadIdx.x, lane = tid & 63, wave = tid >> 6, b = blockIdx.x;
  {
    const int row = b * 4 + wave;
    const u32x4* src = (const u32x4*)(p.x + (size_t)row * D_);
    u32x4 v0 = src[lane], v1 = src[lane + 64];
    float f[16];
    f[0] = bf_lo(v0.x); f[1] = bf_hi(v0.x); f[2] = bf_lo(v0.y); f[3] = bf_hi(v0.y); f[4] = bf_lo(v0.z); f[5] = bf_hi(v0.z); f[6] = bf_lo(v0.w); f[7] = bf_hi(v0.w);
    f[8] = bf_lo(v1.x); f[9] = bf_hi(v1.x); f[10] = bf_lo(v1.y); f[11] = bf_hi(v1.y); f[12] = bf_lo(v1.z); f[13] = bf_hi(v1.z); f[14] = bf_lo(v1.w); f[15] = bf_hi(v1.w);
    float s = 0; for (int i = 0; i < 16; ++i) s += f[i];
    for (int o = 32; o; o >>= 1) s += __shfl_xor(s, o);
    const float mean = s * (1.f / D_);
    float q = 0; for (int i = 0; i < 16; ++i) { f[i] -= mean; q += f[i] * f[i]; }
    for (int o = 32; o; o >>= 1) q += __shfl_xor(q, o);
    const float rs = 1.f / sqrtf(q * (1.f / D_) + 1e-5f);
    u32x4 o0 = {cvt_pk(f[0] * rs, f[1] * rs), cvt_pk(f[2] * rs, f[3] * rs), cvt_pk(f[4] * rs, f[5] * rs), cvt_pk(f[6] * rs, f[7] * rs)};
    u32x4 o1 = {cvt_pk(f[8] * rs, f[9] * rs), cvt_pk(f[10] * rs, f[11] * rs), cvt_pk(f[12] * rs, f[13] * rs), cvt_pk(f[14] * rs, f[15] * rs)};
    u32x4* dst = (u32x4*)(p.ws + WS_XN + (size_t)row * D_ * 2);
    dst[lane] = o0; dst[lane + 64] = o1;
  }
  {
    const u32 off = (u32)(b * 256 + tid) * 16;
    const u32 x = ((off >> 14) << 13) | (off & 8191);
    u32x4 v = {SENT, SENT, SENT, SENT};
    if (x < 32768) {
      const u32 c8 = x >> 8, g = (x >> 7) & 1, r = (x >> 4) & 7;
      if (g == 0) { v = *(const u32x4*)(p.mem0 + r * D_ + c8 * 8); v.x = nosent(v.x); v.y = nosent(v.y); v.z = nosent(v.z); v.w = nosent(v.w); }
      else v = u32x4{0, 0, 0, 0};
    }
    *(u32x4*)(xch_base(p.ws) + off) = v;
  }
  if (b == 0) { u32* ctl = (u32*)(p.ws + WS_CTL); for (int i = tid; i < 4096; i += 256) ctl[i] = 0; }
}

DEV int take_role(u32* ctl, int* s_tmp) {
  if (threadIdx.x == 0) {
    u32 xcc; asm volatile("s_getreg_b32 %0, hwreg(HW_REG_XCC_ID)" : "=s"(xcc)); xcc &= 7;
    const u32 t = ctl_add(ctl + C_TICK + xcc * 32, 1);
    int role;
    if (t < 32) { role = xcc * 32 + t; ctl_add(ctl + C_TOTAL, 1); }
    else {
      const u32 k = ctl_add(ctl + C_OVF, 1); ctl_add(ctl + C_TOTAL, 1);
      int sp = 0; while (ctl_ld(ctl + C_TOTAL) < gridDim.x && ++sp < 2000000) __builtin_amdgcn_s_sleep(2);
      role = -1; u32 n = 0;
      for (int xx = 0; xx < 8; ++xx) { const u32 c = ctl_ld(ctl + C_TICK + xx * 32); if (c < 32) { if (k < n + (32 - c)) { role = xx * 32 + c + (k - n); break; } n += 32 - c; } }
    }
    *s_tmp = role;
  }
  __syncthreads();
  return __builtin_amdgcn_readfirstlane(*s_tmp);
}
DEV float red16_add(float x) { u32 a = __float_as_uint(x); auto r = __builtin_amdgcn_permlane16_swap(a, a, false, false); return __uint_as_float(r[0]) + __uint_as_float(r[1]); }
DEV float red32_add(float x) { u32 a = __float_as_uint(x); auto r = __builtin_amdgcn_permlane32_swap(a, a, false, false); return __uint_as_float(r[0]) + __uint_as_float(r[1]); }
DEV float red16_max(float x) { u32 a = __float_as_uint(x); auto r = __builtin_amdgcn_permlane16_swap(a, a, false, false); return fmaxf(__uint_as_float(r[0]), __uint_as_float(r[1])); }
DEV float red32_max(float x) { u32 a = __float_as_uint(x); auto r = __builtin_amdgcn_permlane32_swap(a, a, false, false); return fmaxf(__uint_as_float(r[0]), __uint_as_float(r[1])); }
DEV f32x4 halfsum4(f32x4 a, f32x4 b) {
  float d0, d1, d2, d3;
  asm volatile("s_nop 7\n s_nop 3\n"
               "v_add_f32_dpp %0, %4, %4 row_ror:8 row_mask:0xf bank_mask:0x3\n v_add_f32_dpp %1, %5, %5 row_ror:8 row_mask:0xf bank_mask:0x3\n"
               "v_add_f32_dpp %2, %6, %6 row_ror:8 row_mask:0xf bank_mask:0x3\n v_add_f32_dpp %3, %7, %7 row_ror:8 row_mask:0xf bank_mask:0x3\n"
               "v_add_f32_dpp %0, %8, %8 row_ror:8 row_mask:0xf bank_mask:0xc\n v_add_f32_dpp %1, %9, %9 row_ror:8 row_mask:0xf bank_mask:0xc\n"
               "v_add_f32_dpp %2, %10, %10 row_ror:8 row_mask:0xf bank_mask:0xc\n v_add_f32_dpp %3, %11, %11 row_ror:8 row_mask:0xf bank_mask:0xc\n s_nop 1"
               : "=&v"(d0), "=&v"(d1), "=&v"(d2), "=&v"(d3)
               : "v"(a[0]), "v"(a[1]), "v"(a[2]), "v"(a[3]), "v"(b[0]), "v"(b[1]), "v"(b[2]), "v"(b[3]));
  return f32x4{d0, d1, d2, d3};
}
DEV u32 nos(u32 x) { return min(x, 0xFFFFFFFEu); }
#define BARL() asm volatile("s_waitcnt lgkmcnt(0)\n s_barrier" ::: "memory")
DEV void nap(int n) { for (int i = 0; i < n; ++i) __builtin_amdgcn_s_sleep(1); }
#define CH_D1 12
#define CH_D3 10
#define CH_D2 8
#define SMW 2
#define CH_INC 1
constexpr int SPIN_CAP = 150000;
constexpr int L_RQK = 0;
constexpr int L_RV = 8 * 584 * 4;
constexpr int L_R2 = L_RV + 8 * 296 * 4;
constexpr int L_KT = L_R2 + 8 * 160 * 4;
constexpr int L_VT = L_KT + 2 * 32 * 64;
constexpr int L_VM = L_VT + 2 * 32 * 64;
constexpr int L_F = L_VM + 16 * 64 + 64;
constexpr int L_W2 = L_F + 1024;
constexpr int L_CHAIN_END = L_VM + 16 * 64;

#define POLL(N, V, LDEXPR, NSP) do { \
    if (dead) { _Pragma("unroll") for (int k = 0; k < N; ++k) V[k] = u32x4{0, 0, 0, 0}; } \
    else for (int sp = 0;; ++sp) { \
      asm volatile("" ::: "memory"); \
      _Pragma("unroll") for (int k = 0; k < N; ++k) V[k] = (LDEXPR); \
      u32 mxv = 0; \
      _Pragma("unroll") for (int k = 0; k < N; ++k) mxv = max(max(mxv, max(V[k].x, V[k].y)), max(V[k].z, V[k].w)); \
      if (__all(mxv != SENT)) { NSP += sp; break; } \
      if (sp > SPIN_CAP || ((sp & 255) == 255 && __builtin_amdgcn_readfirstlane(ctl_ld(ctl + C_TMO)))) { ctl_st(ctl + C_TMO, 1); dead = true; break; } \
    } } while (0)
DEV u32x4 ldx(__amdgpu_buffer_rsrc_t rs, u32 voff, u32 soff) { return __builtin_amdgcn_raw_buffer_load_b128(rs, voff, soff, 16); }
DEV void stx(__amdgpu_buffer_rsrc_t rs, u32 voff, u32 soff, u32x4 v) { __builtin_amdgcn_raw_buffer_store_b128(v, rs, voff, soff, 16); }

DEV void chain_role(const Params& p, const int role, char* smem) {
  const int tid = threadIdx.x, w = __builtin_amdgcn_readfirstlane(tid >> 6), l = tid & 63, q = l >> 4, i = l & 15, r = l & 7, g2 = (l >> 3) & 1;
  const int h = role >> 2, j = role & 3, cs = role >> 1, g = role & 1;
  u32* ctl = (u32*)(p.ws + WS_CTL);
  char* xb = xch_base(p.ws);
  if (role == 0 && w == 0) {
    u32 lat[2] = {1u << 30, 1u << 30};
    for (int rep = 0; rep < 24; ++rep) {
      const int c = rep & 1;
      u32* pq = (u32*)(xb + 127 * 16384 + c * 8192 + 8064) + (l & 31);
      const long long t0 = clock64();
      asm volatile("global_store_dword %0, %1, off sc1\n s_waitcnt vmcnt(0)" ::"v"(pq), "v"(SENT) : "memory");
      const u32 dt = (u32)(clock64() - t0);
      if (c) lat[1] = min(lat[1], dt); else lat[0] = min(lat[0], dt);
    }
    if (l == 0) { ctl[C_NEAR + 1] = lat[0]; ctl[C_NEAR + 2] = lat[1]; ctl_st(ctl + C_NEAR, lat[1] < lat[0] ? 2u : 1u); }
  }
  bf16x8 W1[6][4];
  bf16x8 W2t[8];
  {
    char* stg = smem + w * 8192;
    const u16* mats[3] = {p.Wq, p.Wk, p.Wv};
    u32x4 raw[4][8];
#pragma unroll
    for (int m = 0; m < 3; ++m) {
      const u16* src = mats[m] + (size_t)(128 * w) * HD + h * 128 + 32 * j;
#pragma unroll
      for (int it = 0; it < 8; ++it) raw[m][it] = *(const u32x4*)(src + (size_t)(it * 16 + (l >> 2)) * HD + (l & 3) * 8);
    }
    {
      const u16* src = p.Wo + (size_t)(2048 * g + 256 * w) * D_ + 16 * cs;
#pragma unroll
      for (int it = 0; it < 8; ++it) raw[3][it] = *(const u32x4*)(src + (size_t)(it * 32 + (l >> 1)) * D_ + (l & 1) * 8);
    }
    __builtin_amdgcn_sched_barrier(0);
#pragma unroll
    for (int m = 0; m < 3; ++m) {
#pragma unroll
      for (int it = 0; it < 8; ++it) *(u32x4*)(stg + (it * 16 + (l >> 2)) * 64 + (l & 3) * 16) = raw[m][it];
      asm volatile("s_waitcnt lgkmcnt(0)" ::: "memory");
#pragma unroll
      for (int t = 0; t < 2; ++t)
#pragma unroll
        for (int ks = 0; ks < 4; ++ks) {
          const char* a = stg + (32 * ks + 8 * q + (i >> 2)) * 64 + t * 32 + (i & 3) * 8;
          W1[m * 2 + t][ks] = cat4(trread(a), trread(a + 4 * 64));
        }
      asm volatile("s_waitcnt lgkmcnt(0)" ::: "memory");
    }
#pragma unroll
    for (int it = 0; it < 8; ++it) *(u32x4*)(stg + (it * 32 + (l >> 1)) * 32 + (l & 1) * 16) = raw[3][it];
    asm volatile("s_waitcnt lgkmcnt(0)" ::: "memory");
#pragma unroll
    for (int ks = 0; ks < 8; ++ks) { const char* a = stg + (32 * ks + 8 * q + (i >> 2)) * 32 + (i & 3) * 8; W2t[ks] = cat4(trread(a), trread(a + 4 * 32)); }
    asm volatile("s_waitcnt lgkmcnt(0)" ::: "memory");
  }
  __syncthreads();
#pragma unroll
  for (int ks = 0; ks < 8; ++ks) *(bf16x8*)(smem + L_W2 + (w * 8 + ks) * 1024 + l * 16) = W2t[ks];
  const unsigned long long kvb = (unsigned long long)uni((const u16*)(p.ws + WS_K) + h * 128 + 32 * j);
  const u32x4 rkv = {(u32)__builtin_amdgcn_readfirstlane((int)kvb), (u32)__builtin_amdgcn_readfirstlane((int)(kvb >> 32)), 0x7FFFFFFFu, 0x00020000u};
  const u32 vkv = (u32)((l >> 2) * HD + (l & 3) * 8) * 2u;
  const u32 ldsk = (u32)(size_t)(LDSP char*)(smem + L_KT);
  auto dma = [&](u32 dst, u32 so) { asm volatile("s_mov_b32 m0, %0\n s_nop 0\n buffer_load_dwordx4 %1, %2, %3 offen sc1 lds" ::"s"(dst), "v"(vkv), "s"(rkv), "s"(so) : "memory"); };
  auto prefetch = [&](int s) {
    if ((s & 7) == 0) {
      u32* c = ctl + C_KV + (s >> 3) * 32;
      for (int sp = 0; __builtin_amdgcn_readfirstlane(ctl_ld(c)) < (s ? 32u : 128u); ++sp) { __builtin_amdgcn_s_sleep(4); if (sp > 1000000) { ctl_st(ctl + C_TMO, 1); break; } }
    }
    const u32 d = ldsk + (s & 1) * 2048, so = (u32)s * 32 * HD * 2;
    dma(d, so); dma(d + 1024, so + 16 * HD * 2); dma(d + 4096, so + (u32)(WS_V - WS_K)); dma(d + 4096 + 1024, so + (u32)(WS_V - WS_K) + 16 * HD * 2);
  };
  if (w == 7) { prefetch(0); asm volatile("s_waitcnt vmcnt(0)" ::: "memory"); }
  if (w == 0 && l < 32) *(u32x4*)(smem + L_VM + 512 + l * 16) = u32x4{0, 0, 0, 0};
  if (tid == 0) { int sp = 0; u32 c; while ((c = ctl_ld(ctl + C_NEAR)) == 0 && ++sp < 4000000) __builtin_amdgcn_s_sleep(1); *(volatile u32*)(smem + L_CHAIN_END) = c; }
  __syncthreads();
  const u32 near = (__builtin_amdgcn_readfirstlane(*(volatile u32*)(smem + L_CHAIN_END)) == 2) ? 8192u : 0u;
  const __amdgpu_buffer_rsrc_t rs = __builtin_amdgcn_make_buffer_rsrc((void*)(xb + near), 0, 0x7FFFFFFF, 0x00020000);
  auto PH = [](u32 x) -> u32 { return ((x >> 13) << 14) | (x & 8191); };
  constexpr u32 X_S0 = X_SC + 131072;
  const u32 oM = PH(X_M + 4 * w * 1024 + l * 16), oO = PH(X_O + g * 32768 + (32 * w + 4 * g2 + q) * 128 + r * 16);
  const u32 oS = PH(X_SC + h * 4096 + g2 * 512 + q * 128 + r * 16);
  const u32 oS0 = PH(X_S0 + h * 1024 + ((q >> 1) * 2 + g2) * 256 + (q & 1) * 128 + r * 16);
  const u32 pS0 = PH(X_S0 + h * 1024 + j * 256 + (q & 1) * 128 + r * 16);
  const u32 pO = PH(X_O + role * 512 + q * 128 + r * 16);
  const u32 pM = PH(X_M + (2 * cs + ((l >> 2) & 1)) * 256 + g * 128 + (l >> 3) * 16);
  const u32 jo = j * 1024;
  float* RQK = (float*)(smem + L_RQK); float* RV = (float*)(smem + L_RV); float* R2 = (float*)(smem + L_R2);
  const __amdgpu_buffer_rsrc_t rkm = __builtin_amdgcn_make_buffer_rsrc((void*)((u16*)(p.ws + WS_KM) + h * 128 + 32 * j), 0, 0x7FFFFFFF, 0x00020000);
  bool dead = false; int ns1 = 0, ns2 = 0, ns3 = 0;
  int d1 = (w == 1 ? CH_D1 - 4 : CH_D1), d2 = CH_D2, d3 = CH_D3;
#define TS(k)
  const u32x4 SV = {SENT, SENT, SENT, SENT};

  auto reset = [&](int s) {
    const u32 so = (u32)((s - 2) & 3) * (2 * SLOT);
    stx(rs, oS, so + jo, SV); if (l < 32 && g2 == 0) stx(rs, pS0, so, SV);
    if (g2 == 0) stx(rs, pO, so, SV);
    if ((l & 3) == 0) stx(rs, pM, so, SV);
  };
#define RESET() do { if (w == 6 && s >= 2) reset(s); } while (0)
#define PFPOS 3
#define RSPOS 0
  for (int s = 0; s < NSEG; ++s) {
    const u32 sb = (u32)(s & 3) * (2 * SLOT);
    u32x4 mv[4];
    if (s) nap(d1);
    TS(0);
    f32x4 acc[6];
    const f32x4 z4 = {0, 0, 0, 0};
    {
#pragma unroll
      for (int k = 0; k < 4; ++k) mv[k] = ldx(rs, oM + k * 1024, sb);
      if (PFPOS == 4 && w == 7 && s + 1 < NSEG) prefetch(s + 1);
#pragma unroll
      for (int ks = 0; ks < 4; ++ks)
#pragma unroll
        for (int t = 0; t < 4; ++t) acc[t] = mfma16(W1[t][ks], asfrag(mv[ks]), ks ? acc[t] : z4);
      u32 mxv = 0;
#pragma unroll
      for (int k = 0; k < 4; ++k) mxv = max(max(mxv, max(mv[k].x, mv[k].y)), max(mv[k].z, mv[k].w));
      if (!__all(mxv != SENT)) {
        ++ns1; d1 = min(d1 + CH_INC, CH_D1 + 10);
        POLL(4, mv, ldx(rs, oM + k * 1024, sb), ns1);
#pragma unroll
        for (int ks = 0; ks < 4; ++ks)
#pragma unroll
          for (int t = 0; t < 4; ++t) acc[t] = mfma16(W1[t][ks], asfrag(mv[ks]), ks ? acc[t] : z4);
      }
    }
    TS(1);
    {
      f32x4 a, b;
      a = halfsum4(acc[0], acc[2]); b = halfsum4(acc[1], acc[3]);
      float* d = RQK + w * 584 + g2 * 288 + r * 36 + 4 * q;
      *(f32x4*)d = a; *(f32x4*)(d + 16) = b;
    }
    BARL();
    TS(2);
    if (PFPOS == 0 && w == 7 && s + 1 < NSEG) prefetch(s + 1);
    if (RSPOS == 0) RESET();
    {
      const float* b = RQK + (l >> 5) * 288 + w * 36 + (l & 31);
      float v[8];
#pragma unroll
      for (int ww = 0; ww < 8; ++ww) v[ww] = b[ww * 584];
      __builtin_amdgcn_sched_barrier(0);
      const float t = ((v[0] + v[1]) + (v[2] + v[3])) + ((v[4] + v[5]) + (v[6] + v[7]));
      const float o = __int_as_float(__builtin_amdgcn_update_dpp(0, __float_as_int(t), 0xB1, 0xF, 0xF, true));
      if ((l & 1) == 0) *(u32*)(smem + L_F + (l >> 5) * 512 + w * 64 + (l & 31) * 2) = cvt_pk(t, o);
    }
    BARL();
    if (PFPOS == 3 && w == 7 && s + 1 < NSEG) prefetch(s + 1);
    if (RSPOS == 3) RESET();
    if (w == 0) {
      const u32x4 qf = *(const u32x4*)(smem + L_F + r * 64 + q * 16);
      const u32x4 kfrag = *(const u32x4*)(smem + L_F + 512 + r * 64 + q * 16);
      const char* kt = smem + L_KT + (s & 1) * 2048 + i * 64 + q * 16;
      const bf16x8 kt0 = *(const bf16x8*)kt, kt1 = *(const bf16x8*)(kt + 1024);
      const f32x4 z = {0, 0, 0, 0};
      const f32x4 c1 = mfma16(kt0, asfrag(qf), z);
      const f32x4 c2 = mfma16(kt1, asfrag(qf), z);
      const f32x4 c0 = mfma16(asfrag(kfrag), asfrag(qf), z);
      if (s < NSEG - 1) {
        const u32x4 v12 = {nos(__float_as_uint(g2 ? c2[0] : c1[0])), nos(__float_as_uint(g2 ? c2[1] : c1[1])), nos(__float_as_uint(g2 ? c2[2] : c1[2])), nos(__float_as_uint(g2 ? c2[3] : c1[3]))};
        stx(rs, oS, sb + jo, v12);
        const u32x4 v0 = {nos(__float_as_uint(c0[0])), nos(__float_as_uint(c0[1])), nos(__float_as_uint(c0[2])), nos(__float_as_uint(c0[3]))};
        if (l < 32 && g2 == 0) stx(rs, pS0, sb, v0);
      }
      if (g2 == 0) __builtin_amdgcn_raw_buffer_store_b128(kfrag, rkm, (r * HD + 8 * q) * 2, s * 8 * HD * 2, 16);
    }
    TS(3);
#pragma unroll
    for (int ks = 0; ks < 4; ++ks) { acc[4] = mfma16(W1[4][ks], asfrag(mv[ks]), ks ? acc[4] : z4); acc[5] = mfma16(W1[5][ks], asfrag(mv[ks]), ks ? acc[5] : z4); }
    {
      f32x4 a;
      a = halfsum4(acc[4], acc[5]);
      *(f32x4*)(RV + w * 296 + r * 36 + 16 * g2 + 4 * q) = a;
    }
    u32x4 sv[5];
    if (w == SMW && s < NSEG - 1) {
      nap(d2);
#pragma unroll
      for (int k = 0; k < 5; ++k) sv[k] = k < 4 ? ldx(rs, oS + k * 1024, sb) : ldx(rs, oS0, sb);
    }
    BARL();
    if (PFPOS == 2 && w == 7 && s + 1 < NSEG) prefetch(s + 1);
    if (RSPOS == 2) RESET();
    TS(4);
    if (w == SMW) {
      f32x4 rd[4][2];
#pragma unroll
      for (int ww = 0; ww < 4; ++ww) { const float* b = RV + (4 * g2 + ww) * 296 + r * 36 + 8 * q; rd[ww][0] = *(const f32x4*)b; rd[ww][1] = *(const f32x4*)(b + 4); }
      __builtin_amdgcn_sched_barrier(0);
      f32x4 s0 = (rd[0][0] + rd[1][0]) + (rd[2][0] + rd[3][0]), s1 = (rd[0][1] + rd[1][1]) + (rd[2][1] + rd[3][1]);
#pragma unroll
      for (int e = 0; e < 4; ++e) { s0[e] += ror8(s0[e]); s1[e] += ror8(s1[e]); }
      const u32x4 vf = {cvt_pk(s0[0], s0[1]), cvt_pk(s0[2], s0[3]), cvt_pk(s1[0], s1[1]), cvt_pk(s1[2], s1[3])};
      if (g2 == 0) *(u32x4*)(smem + L_VM + r * 64 + q * 16) = vf;
      if (s < NSEG - 1) {
        const char* vt = smem + L_VT + (s & 1) * 2048 + (4 * q + (i >> 2)) * 64 + (i & 3) * 16;
        const char* vm = smem + L_VM + (4 * q + (i >> 2)) * 64 + (i & 3) * 16;
        const bf16x8 vt0 = cat4(trread(vt), trread(vt + 1024)), vt1 = cat4(trread(vt + 8), trread(vt + 1024 + 8));
        const s16x4 zz = {0, 0, 0, 0};
        const bf16x8 vm0 = cat4(trread(vm), zz), vm1 = cat4(trread(vm + 8), zz);
        TS(5);
        {
          u32 mxv = 0;
#pragma unroll
          for (int k = 0; k < 5; ++k) mxv = max(max(mxv, max(sv[k].x, sv[k].y)), max(sv[k].z, sv[k].w));
          if (!__all(mxv != SENT)) { ++ns2; d2 = min(d2 + CH_INC, CH_D2 + 10); POLL(5, sv, (k < 4 ? ldx(rs, oS + k * 1024, sb) : ldx(rs, oS0, sb)), ns2); }
        }
        TS(6);
        const float sc = 0.08838834764831845f * 1.4426950408889634f;
        float st[4], sm[4];
        st[0] = (__uint_as_float(sv[0].x) + __uint_as_float(sv[1].x)) + (__uint_as_float(sv[2].x) + __uint_as_float(sv[3].x));
        st[1] = (__uint_as_float(sv[0].y) + __uint_as_float(sv[1].y)) + (__uint_as_float(sv[2].y) + __uint_as_float(sv[3].y));
        st[2] = (__uint_as_float(sv[0].z) + __uint_as_float(sv[1].z)) + (__uint_as_float(sv[2].z) + __uint_as_float(sv[3].z));
        st[3] = (__uint_as_float(sv[0].w) + __uint_as_float(sv[1].w)) + (__uint_as_float(sv[2].w) + __uint_as_float(sv[3].w));
        sm[0] = __uint_as_float(sv[4].x); sm[1] = __uint_as_float(sv[4].y); sm[2] = __uint_as_float(sv[4].z); sm[3] = __uint_as_float(sv[4].w);
#pragma unroll
        for (int e = 0; e < 4; ++e) sm[e] = red32_add(sm[e] + ror8(sm[e]));
        float mx = fmaxf(fmaxf(fmaxf(st[0], st[1]), fmaxf(st[2], st[3])), fmaxf(fmaxf(sm[0], sm[1]), fmaxf(sm[2], sm[3])));
        mx = fmaxf(mx, ror8(mx)); mx = red32_max(red16_max(mx));
        const float nm = -mx * sc;
#pragma unroll
        for (int e = 0; e < 4; ++e) { st[e] = __builtin_amdgcn_exp2f(fmaf(st[e], sc, nm)); sm[e] = __builtin_amdgcn_exp2f(fmaf(sm[e], sc, nm)); }
        const u32 p0 = cvt_pk(st[0], st[1]), p1 = cvt_pk(st[2], st[3]);
        const u32 m0 = cvt_pk(sm[0], sm[1]), m1 = cvt_pk(sm[2], sm[3]);
        const u32x4 pt = {p0, p1, ror8u(p0), ror8u(p1)};
        const u32x4 pm = {q < 2 ? m0 : 0u, q < 2 ? m1 : 0u, 0u, 0u};
        const f32x4 z = {0, 0, 0, 0};
        const f32x4 o0 = mfma16(vm0, asfrag(pm), mfma16(vt0, asfrag(pt), z)), o1 = mfma16(vm1, asfrag(pm), mfma16(vt1, asfrag(pt), z));
        float ts = (bf_lo(p0) + bf_hi(p0)) + (bf_lo(p1) + bf_hi(p1)), ms = (bf_lo(m0) + bf_hi(m0)) + (bf_lo(m1) + bf_hi(m1));
        ts += ror8(ts); ts = red32_add(red16_add(ts)); ms = red16_add(ms);
        const float inv = __builtin_amdgcn_rcpf(ts + ms);
        const u32x4 ov = {nos(cvt_pk(o0[0] * inv, o0[1] * inv)), nos(cvt_pk(o0[2] * inv, o0[3] * inv)), nos(cvt_pk(o1[0] * inv, o1[1] * inv)), nos(cvt_pk(o1[2] * inv, o1[3] * inv))};
        if (g2 == 0) stx(rs, pO, sb, ov);
      }
      if (g2 == 0) __builtin_amdgcn_raw_buffer_store_b128(vf, rkm, (4u << 20) + (r * HD + 8 * q) * 2, s * 8 * HD * 2, 16);
    }
    TS(7);
    if (s == NSEG - 1) {
      asm volatile("s_waitcnt vmcnt(0)" ::: "memory");
      BARL();
      if (tid == 0) ctl_add(ctl + C_KM + s * 32 + h, 1);
      break;
    }
    BARL();
    if (PFPOS == 1 && w == 7) prefetch(s + 1);
    if (RSPOS == 1) RESET();
    u32x4 ov[4];
    bf16x8 W2[8];
#pragma unroll
    for (int ks = 0; ks < 8; ++ks) W2[ks] = *(const bf16x8*)(smem + L_W2 + (w * 8 + ks) * 1024 + l * 16);
    nap(d3); TS(8);
    f32x4 a0, a1;
    {
#pragma unroll
      for (int k = 0; k < 4; ++k) ov[k] = ldx(rs, oO + k * 1024, sb);
#pragma unroll
      for (int k = 0; k < 4; ++k) { a0 = mfma16(W2[2 * k], asfrag(ov[k]), k ? a0 : z4); a1 = mfma16(W2[2 * k + 1], asfrag(ov[k]), k ? a1 : z4); }
      u32 mxv = 0;
#pragma unroll
      for (int k = 0; k < 4; ++k) mxv = max(max(mxv, max(ov[k].x, ov[k].y)), max(ov[k].z, ov[k].w));
      if (!__all(mxv != SENT)) {
        ++ns3; d3 = min(d3 + CH_INC, CH_D3 + 10);
        POLL(4, ov, ldx(rs, oO + k * 1024, sb), ns3);
#pragma unroll
        for (int k = 0; k < 4; ++k) { a0 = mfma16(W2[2 * k], asfrag(ov[k]), k ? a0 : z4); a1 = mfma16(W2[2 * k + 1], asfrag(ov[k]), k ? a1 : z4); }
      }
    }
    TS(9);
    {
      f32x4 a;
#pragma unroll
      for (int e = 0; e < 4; ++e) a[e] = a0[e] + ror8(a1[e]);
      if (g2 == 0) *(f32x4*)(R2 + w * 160 + r * 20 + 4 * q) = a;
    }
    asm volatile("s_waitcnt vmcnt(0)" ::: "memory");
    BARL();
    if (tid == 192) ctl_add(ctl + C_KM + s * 32 + h, 1);
    TS(10);
    if (w == 1) {
      const int n2 = l & 7, rr = l >> 3;
      f32x2 rr8[8];
#pragma unroll
      for (int ww = 0; ww < 8; ++ww) rr8[ww] = *(const f32x2*)(R2 + ww * 160 + rr * 20 + 2 * n2);
      __builtin_amdgcn_sched_barrier(0);
      const f32x2 sum = ((rr8[0] + rr8[1]) + (rr8[2] + rr8[3])) + ((rr8[4] + rr8[5]) + (rr8[6] + rr8[7]));
      const u32 v = nos(cvt_pk(sum[0], sum[1]));
      const u32x4 o = {(u32)__builtin_amdgcn_update_dpp(0, (int)v, 0x00, 0xF, 0xF, true), (u32)__builtin_amdgcn_update_dpp(0, (int)v, 0x55, 0xF, 0xF, true),
                       (u32)__builtin_amdgcn_update_dpp(0, (int)v, 0xAA, 0xF, 0xF, true), (u32)__builtin_amdgcn_update_dpp(0, (int)v, 0xFF, 0xF, 0xF, true)};
      if ((l & 3) == 0) stx(rs, pM, (u32)((s + 1) & 3) * (2 * SLOT), o);
    }
  }
}
constexpr int G_HT = 128 * 64;
DEV int lds_byte(int r, int c) { int st = (r >> 4) * 2 + (c >> 5), rr = r & 15, cc = c & 31, ob = rr * 64 + cc * 2; return st * 1024 + (ob ^ (((ob >> 9) & 1) << 5)); }
DEV void stage_rc(int b, int& R, int& C) { int st = b / 1024, sb = b % 1024, swz = sb ^ (((sb >> 9) & 1) << 5); R = (st >> 1) * 16 + swz / 64; C = (st & 1) * 32 + (swz % 64) / 2; }
DEV int swzb(int row) { return ((row & 3) << 2) | ((row >> 2) & 3); }
DEV float mish(float h) {
  const float u = __expf(-fabsf(h));
  const float n = h >= 0.f ? 1.f + 2.f * u : u * (u + 2.f);
  const float d = h >= 0.f ? n + 2.f * u * u : n + 2.f;
  return h * n * __builtin_amdgcn_rcpf(d);
}
DEV void gemm256(const int wv, const int MODE, const u16* __restrict__ A, const int lda, const u16* __restrict__ B, const int ldb, const int K, void* C, const int ldc, const u16* bias, char* pbuf, u32* pair, char* smem) {
  u16* shm = (u16*)smem;
#define SA(b, h) (shm + ((b) * 2 + (h)) * G_HT)
#define SB(b, h) (shm + (4 + (b) * 2 + (h)) * G_HT)
#define STA_X(T, VO, P, br, kt) do { const int _s = ((br) * lda + (kt) * 64) * 2; \
    __builtin_amdgcn_raw_ptr_buffer_load_lds(rsa, (LDSP void*)((char*)(P) + (T) * 16), 16, (VO), _s, 0, 0); \
    __builtin_amdgcn_raw_ptr_buffer_load_lds(rsa, (LDSP void*)((char*)(P) + (T) * 16 + 8192), 16, (VO), _s + 128 * lda, 0, 0); } while (0)
#define STB_X(T, VO, P, bc, kt) do { const int _s = ((kt) * 64 * ldb + (bc)) * 2; \
    __builtin_amdgcn_raw_ptr_buffer_load_lds(rsb, (LDSP void*)((char*)(P) + (T) * 16), 16, (VO), _s, 0, 0); \
    __builtin_amdgcn_raw_ptr_buffer_load_lds(rsb, (LDSP void*)((char*)(P) + (T) * 16 + 8192), 16, (VO), _s + 64 * ldb, 0, 0); } while (0)
#define STA(P, br, kt) STA_X(t0, voa, P, br, kt)
#define STB(P, bc, kt) STB_X(t0, vob, P, bc, kt)
#define LDA(dst, b, h) for (int m = 0; m < 4; ++m) for (int k = 0; k < 2; ++k) dst[m][k] = *reinterpret_cast<const bf16x8*>((char*)SA(b, h) + lds_byte(wr * 64 + m * 16 + fr, k * 32 + fq * 8))
#define LDB(dst, b, h) for (int n = 0; n < 2; ++n) for (int k = 0; k < 2; ++k) { const char* _p = (const char*)SB(b, h) + k * 8192; \
      dst[n][k] = cat4(trread(_p + bo[n][0]), trread(_p + bo[n][1])); }
#define MMA(ai, bj, At, Bt) do { __builtin_amdgcn_s_setprio(1); \
    for (int m = 0; m < 4; ++m) for (int n = 0; n < 2; ++n) for (int k = 0; k < 2; ++k) acc[ai][bj][m][n] = mfma16(Bt[n][k], At[m][k], acc[ai][bj][m][n]); \
    __builtin_amdgcn_s_setprio(0); } while (0)
#define WAIT_V(n) asm volatile("s_waitcnt vmcnt(" #n ")" ::: "memory")
#define WAIT_L(n) asm volatile("s_waitcnt lgkmcnt(" #n ")" ::: "memory")
#define BAR __builtin_amdgcn_s_barrier()
#define SCHED __builtin_amdgcn_sched_barrier(0)
  const int t0 = tid_of(wv);
  int wid = t0 >> 6, lane = t0 & 63, wr = wid >> 2, wc = wid & 3, fr = lane & 15, fq = lane >> 4;
  f32x4 acc[2][2][4][2] = {};
  bf16x8 At[4][2], B0[2][2], B1[2][2];
  int bo[2][2];
  for (int n = 0; n < 2; ++n) for (int t = 0; t < 2; ++t) { const int row = 8 * fq + 4 * t + (fr >> 2), ch = 4 * wc + (fr & 3); bo[n][t] = 256 * row + 16 * (ch ^ swzb(row)) + 8 * n; }
  const int nt = K / 64;
  const __amdgpu_buffer_rsrc_t rsa = __builtin_amdgcn_make_buffer_rsrc((void*)uni(A), 0, 0x7FFFFFFF, 0x00020000), rsb = __builtin_amdgcn_make_buffer_rsrc((void*)uni(B), 0, 0x7FFFFFFF, 0x00020000);
  u32 voa, vob;
  { int _r, _c; stage_rc(t0 * 16, _r, _c); voa = (u32)(_r * lda + _c) * 2u; const int _b = t0 * 16, _rb = _b >> 8; vob = (u32)(_rb * ldb + ((((_b >> 4) & 15) ^ swzb(_rb)) * 8)) * 2u; }
  STB(SB(0, 0), 0, 0); STA(SA(0, 0), 0, 0); STB(SB(0, 1), 128, 0); STA(SA(0, 1), 128, 0);
  if (wr == 1) BAR;
  WAIT_V(4); BAR;
  STB(SB(1, 0), 0, 1); STA(SA(1, 0), 0, 1); STB(SB(1, 1), 128, 1);
  WAIT_V(6); BAR;
  for (int t = 0; t < nt - 2; t += 2) {
    LDB(B0, 0, 0); SCHED; LDA(At, 0, 0); STA(SA(1, 1), 128, t + 1);
    WAIT_L(8); BAR; WAIT_L(0); MMA(0, 0, At, B0); BAR; SCHED;
    LDB(B1, 0, 1); STB(SB(0, 0), 0, t + 2);
    BAR; WAIT_L(0); MMA(0, 1, At, B1); BAR;
    LDA(At, 0, 1); STA(SA(0, 0), 0, t + 2);
    BAR; WAIT_L(0); MMA(1, 0, At, B0); BAR; SCHED;
    STB(SB(0, 1), 128, t + 2);
    WAIT_V(6); BAR; MMA(1, 1, At, B1); BAR;
    LDB(B0, 1, 0); SCHED; LDA(At, 1, 0); STA(SA(0, 1), 128, t + 2);
    WAIT_L(8); BAR; WAIT_L(0); MMA(0, 0, At, B0); BAR; SCHED;
    LDB(B1, 1, 1); STB(SB(1, 0), 0, t + 3);
    BAR; WAIT_L(0); MMA(0, 1, At, B1); BAR;
    LDA(At, 1, 1); STA(SA(1, 0), 0, t + 3);
    BAR; WAIT_L(0); MMA(1, 0, At, B0); BAR; SCHED;
    STB(SB(1, 1), 128, t + 3);
    WAIT_V(6); BAR; MMA(1, 1, At, B1); BAR;
  }
  int tz = tid_of(wv); wid = tz >> 6; lane = tz & 63; wr = wid >> 2; wc = wid & 3; fr = lane & 15; fq = lane >> 4;
  { LDB(B0, 0, 0); WAIT_V(0); LDA(At, 0, 0); { int _r, _c; stage_rc(tz * 16, _r, _c); const u32 voa2 = (u32)(_r * lda + _c) * 2u; STA_X(tz, voa2, SA(1, 1), 128, nt - 1); }
    BAR; WAIT_L(0); MMA(0, 0, At, B0); BAR;
    LDB(B1, 0, 1); BAR; WAIT_L(0); MMA(0, 1, At, B1); BAR;
    LDA(At, 0, 1); WAIT_V(4); BAR; WAIT_L(0); MMA(1, 0, At, B0); MMA(1, 1, At, B1); BAR; }
  { LDB(B0, 1, 0); LDA(At, 1, 0); WAIT_V(2); BAR; WAIT_L(0); MMA(0, 0, At, B0); BAR;
    LDB(B1, 1, 1); WAIT_V(0); BAR; WAIT_L(0); MMA(0, 1, At, B1); BAR;
    LDA(At, 1, 1); BAR; WAIT_L(0); MMA(1, 0, At, B0); MMA(1, 1, At, B1); BAR; }
  if (wr == 0) BAR;
  const __amdgpu_buffer_rsrc_t rc = __builtin_amdgcn_make_buffer_rsrc((void*)uni((char*)C), 0, 0x7FFFFFFF, 0x00020000);
  const __amdgpu_buffer_rsrc_t rp = __builtin_amdgcn_make_buffer_rsrc((void*)uni(pbuf), 0, 0x7FFFFFFF, 0x00020000);
#pragma unroll
  for (int ai = 0; ai < 2; ++ai)
#pragma unroll
  for (int bj = 0; bj < 2; ++bj)
#pragma unroll
  for (int m = 0; m < 4; ++m) {
    const int row = ai * 128 + wr * 64 + m * 16 + fr, col = bj * 128 + wc * 32 + fq * 8;
    f32x4 v0 = acc[ai][bj][m][0], v1 = acc[ai][bj][m][1];
    if (MODE >= 4) {
      if (MODE == 5) {
        const __amdgpu_buffer_rsrc_t ry = __builtin_amdgcn_make_buffer_rsrc((void*)bias, 0, 0x7FFFFFFF, 0x00020000);
        const u32 o = (u32)(row * ldc + col) * 4;
        v0 += __builtin_bit_cast(f32x4, __builtin_amdgcn_raw_buffer_load_b128(ry, o, 0, 16)) + __builtin_bit_cast(f32x4, __builtin_amdgcn_raw_buffer_load_b128(ry, o, 8u << 20, 16));
        v1 += __builtin_bit_cast(f32x4, __builtin_amdgcn_raw_buffer_load_b128(ry, o + 16, 0, 16)) + __builtin_bit_cast(f32x4, __builtin_amdgcn_raw_buffer_load_b128(ry, o + 16, 8u << 20, 16));
        acc[ai][bj][m][0] = v0; acc[ai][bj][m][1] = v1;
      }
      __builtin_amdgcn_raw_buffer_store_b128(__builtin_bit_cast(u32x4, v0), rp, tz * 16 + (((ai * 2 + bj) * 4 + m) * 2) * 8192, (MODE & 1) * 262144, 16);
      __builtin_amdgcn_raw_buffer_store_b128(__builtin_bit_cast(u32x4, v1), rp, tz * 16 + (((ai * 2 + bj) * 4 + m) * 2 + 1) * 8192, (MODE & 1) * 262144, 16);
    } else if (MODE >= 2) {
      if (MODE == 2) { const u32x4 xv = *(const u32x4*)(bias + (long)row * ldc + col);
        v0[0] += bf_lo(xv.x); v0[1] += bf_hi(xv.x); v0[2] += bf_lo(xv.y); v0[3] += bf_hi(xv.y); v1[0] += bf_lo(xv.z); v1[1] += bf_hi(xv.z); v1[2] += bf_lo(xv.w); v1[3] += bf_hi(xv.w); }
      __builtin_amdgcn_raw_buffer_store_b128(__builtin_bit_cast(u32x4, v0), rc, (row * ldc + col) * 4, 0, 16);
      __builtin_amdgcn_raw_buffer_store_b128(__builtin_bit_cast(u32x4, v1), rc, (row * ldc + col) * 4 + 16, 0, 16);
    } else {
      if (MODE == 1) { const u32x4 bb = *(const u32x4*)(bias + col);
        v0[0] = mish(v0[0] + bf_lo(bb.x)); v0[1] = mish(v0[1] + bf_hi(bb.x)); v0[2] = mish(v0[2] + bf_lo(bb.y)); v0[3] = mish(v0[3] + bf_hi(bb.y));
        v1[0] = mish(v1[0] + bf_lo(bb.z)); v1[1] = mish(v1[1] + bf_hi(bb.z)); v1[2] = mish(v1[2] + bf_lo(bb.w)); v1[3] = mish(v1[3] + bf_hi(bb.w)); }
      __builtin_amdgcn_raw_buffer_store_b128(u32x4{cvt_pk(v0[0], v0[1]), cvt_pk(v0[2], v0[3]), cvt_pk(v1[0], v1[1]), cvt_pk(v1[2], v1[3])}, rc, (row * ldc + col) * 2, 0, 16);
    }
  }
  if (MODE >= 4) {
    asm volatile("s_waitcnt vmcnt(0)" ::: "memory");
    __syncthreads();
    volatile int* s_last = (volatile int*)(smem + 128 * 1024 + 32);
    if (tz == 0) *s_last = (int)ctl_add(pair, 1);
    __syncthreads();
    if (__builtin_amdgcn_readfirstlane(*s_last) == 1) {
#pragma unroll
      for (int ai = 0; ai < 2; ++ai)
#pragma unroll
      for (int bj = 0; bj < 2; ++bj)
#pragma unroll
      for (int m = 0; m < 4; ++m) {
        const int row = ai * 128 + wr * 64 + m * 16 + fr, col = bj * 128 + wc * 32 + fq * 8;
        const f32x4 v0 = acc[ai][bj][m][0] + __builtin_bit_cast(f32x4, __builtin_amdgcn_raw_buffer_load_b128(rp, tz * 16 + (((ai * 2 + bj) * 4 + m) * 2) * 8192, ((MODE & 1) ^ 1) * 262144, 16));
        const f32x4 v1 = acc[ai][bj][m][1] + __builtin_bit_cast(f32x4, __builtin_amdgcn_raw_buffer_load_b128(rp, tz * 16 + (((ai * 2 + bj) * 4 + m) * 2 + 1) * 8192, ((MODE & 1) ^ 1) * 262144, 16));
        __builtin_amdgcn_raw_buffer_store_b128(u32x4{cvt_pk(v0[0], v0[1]), cvt_pk(v0[2], v0[3]), cvt_pk(v1[0], v1[1]), cvt_pk(v1[2], v1[3])}, rc, (row * ldc + col) * 2, 0, 0);
      }
    }
  }
#undef SA
#undef SB
#undef LDA
#undef LDB
#undef MMA
}
DEV void wait_cnts(const int wv, u32* ctl, int ia, int na, u32 ta, int ib, int nb, u32 tb);
DEV void attn_item(const int wv, const Params& p, int s, int h, char* lds) {
  const int t0 = tid_of(0);
  const int l = t0 & 63, q = l >> 4, i = l & 15;
  const __amdgpu_buffer_rsrc_t rq = __builtin_amdgcn_make_buffer_rsrc((void*)((const u16*)(p.ws + WS_Q) + (size_t)s * 32 * HD + h * 128), 0, 0x7FFFFFFF, 0x00020000);
  const __amdgpu_buffer_rsrc_t rm = __builtin_amdgcn_make_buffer_rsrc((void*)((const u16*)(p.ws + WS_KM) + (size_t)s * 8 * HD + h * 128), 0, 0x7FFFFFFF, 0x00020000);
  const __amdgpu_buffer_rsrc_t rk = __builtin_amdgcn_make_buffer_rsrc((void*)((const u16*)(p.ws + WS_K) + (size_t)s * 32 * HD + h * 128), 0, 0x7FFFFFFF, 0x00020000);
  u32x4 vv[8]; bf16x8 qf[4][2], kf[4][2];
#pragma unroll
  for (int it = 0; it < 8; ++it) vv[it] = __builtin_amdgcn_raw_buffer_load_b128(rk, ((it * 4 + q) * HD + i * 8) * 2, (u32)(WS_V - WS_K), 16);
#pragma unroll
  for (int ks = 0; ks < 4; ++ks) {
    const int e = ks * 32 + q * 8;
    qf[ks][0] = asfrag(__builtin_amdgcn_raw_buffer_load_b128(rq, (i * HD + e) * 2, 0, 16)); qf[ks][1] = asfrag(__builtin_amdgcn_raw_buffer_load_b128(rq, ((16 + i) * HD + e) * 2, 0, 16));
    kf[ks][0] = asfrag(__builtin_amdgcn_raw_buffer_load_b128(rk, (i * HD + e) * 2, 0, 16)); kf[ks][1] = asfrag(__builtin_amdgcn_raw_buffer_load_b128(rk, ((16 + i) * HD + e) * 2, 0, 16));
  }
  __builtin_amdgcn_sched_barrier(0);
#pragma unroll
  for (int it = 0; it < 8; ++it) { const int row = 16 + it * 4 + q; *(u32x4*)(lds + row * 256 + 16 * (i ^ swzb(row))) = vv[it]; }
#pragma unroll
  for (int it = 2; it < 4; ++it) { const int row = it * 4 + q; *(u32x4*)(lds + row * 256 + 16 * (i ^ swzb(row))) = u32x4{0, 0, 0, 0}; }
  f32x4 c[3][2];
#pragma unroll
  for (int t = 0; t < 3; ++t) c[t][0] = c[t][1] = f32x4{0, 0, 0, 0};
#pragma unroll
  for (int ks = 0; ks < 4; ++ks) { c[1][0] = mfma16(kf[ks][0], qf[ks][0], c[1][0]); c[1][1] = mfma16(kf[ks][0], qf[ks][1], c[1][1]); c[2][1] = mfma16(kf[ks][1], qf[ks][1], c[2][1]); }
  wait_cnts(wv, (u32*)(p.ws + WS_CTL), C_KM + s * 32 + (h - wv), 8, 4, 0, 0, 0);
  __syncthreads();
  {
    u32x4 vm[2]; bf16x8 km[4];
#pragma unroll
    for (int it = 0; it < 2; ++it) vm[it] = __builtin_amdgcn_raw_buffer_load_b128(rm, (4u << 20) + ((it * 4 + q) * HD + i * 8) * 2, 0, 16);
#pragma unroll
    for (int ks = 0; ks < 4; ++ks) km[ks] = asfrag(__builtin_amdgcn_raw_buffer_load_b128(rm, ((i & 7) * HD + ks * 32 + q * 8) * 2, 0, 16));
#pragma unroll
    for (int it = 0; it < 2; ++it) { const int row = it * 4 + q; *(u32x4*)(lds + row * 256 + 16 * (i ^ swzb(row))) = vm[it]; }
#pragma unroll
    for (int ks = 0; ks < 4; ++ks) { c[0][0] = mfma16(km[ks], qf[ks][0], c[0][0]); c[0][1] = mfma16(km[ks], qf[ks][1], c[0][1]); }
  }
  const float sc = 0.08838834764831845f * 1.4426950408889634f, NEG = -3.0e38f;
  u32x4 pt[2], pm[2];
#pragma unroll
  for (int u = 0; u < 2; ++u) {
    float sm[4], s1[4], s2[4]; float mx = NEG;
#pragma unroll
    for (int e = 0; e < 4; ++e) {
      sm[e] = q < 2 ? c[0][u][e] * sc : NEG;
      s1[e] = (u == 1 || 4 * q + e <= i) ? c[1][u][e] * sc : NEG;
      s2[e] = (u == 1 && 4 * q + e <= i) ? c[2][u][e] * sc : NEG;
      mx = fmaxf(mx, fmaxf(sm[e], fmaxf(s1[e], s2[e])));
    }
    mx = red32_max(red16_max(mx));
    float sum = 0;
#pragma unroll
    for (int e = 0; e < 4; ++e) {
      sm[e] = q < 2 ? __builtin_amdgcn_exp2f(sm[e] - mx) : 0.f;
      s1[e] = (u == 1 || 4 * q + e <= i) ? __builtin_amdgcn_exp2f(s1[e] - mx) : 0.f;
      s2[e] = (u == 1 && 4 * q + e <= i) ? __builtin_amdgcn_exp2f(s2[e] - mx) : 0.f;
      sum += sm[e] + s1[e] + s2[e];
    }
    sum = red32_add(red16_add(sum));
    const float inv = 1.f / sum;
    pt[u] = u32x4{cvt_pk(s1[0] * inv, s1[1] * inv), cvt_pk(s1[2] * inv, s1[3] * inv), cvt_pk(s2[0] * inv, s2[1] * inv), cvt_pk(s2[2] * inv, s2[3] * inv)};
    pm[u] = u32x4{cvt_pk(sm[0] * inv, sm[1] * inv), cvt_pk(sm[2] * inv, sm[3] * inv), 0u, 0u};
  }
  asm volatile("s_waitcnt lgkmcnt(0)" ::: "memory");
  u16* O = (u16*)(p.ws + WS_O) + (size_t)s * 32 * HD + h * 128;
  const __amdgpu_buffer_rsrc_t ro = __builtin_amdgcn_make_buffer_rsrc((void*)O, 0, 0x7FFFFFFF, 0x00020000);
  const s16x4 zz = {0, 0, 0, 0};
#pragma unroll
  for (int cp = 0; cp < 4; ++cp) {
    f32x4 o[2][2];
#pragma unroll
    for (int c2 = 0; c2 < 2; ++c2) {
      const int ch = 4 * cp + (i & 3);
      const int rm = 4 * q + (i >> 2), r1 = 16 + rm, r2 = 32 + rm;
      const bf16x8 vm = cat4(trread(lds + rm * 256 + 16 * (ch ^ swzb(rm)) + 8 * c2), zz);
      const bf16x8 vt = cat4(trread(lds + r1 * 256 + 16 * (ch ^ swzb(r1)) + 8 * c2), trread(lds + r2 * 256 + 16 * (ch ^ swzb(r2)) + 8 * c2));
#pragma unroll
      for (int u = 0; u < 2; ++u) o[c2][u] = mfma16(vm, asfrag(pm[u]), mfma16(vt, asfrag(pt[u]), f32x4{0, 0, 0, 0}));
    }
#pragma unroll
    for (int u = 0; u < 2; ++u)
      __builtin_amdgcn_raw_buffer_store_b128(u32x4{cvt_pk(o[0][u][0], o[0][u][1]), cvt_pk(o[0][u][2], o[0][u][3]), cvt_pk(o[1][u][0], o[1][u][1]), cvt_pk(o[1][u][2], o[1][u][3])}, ro, ((16 * u + i) * HD + 32 * cp + 8 * q) * 2, 0, 16);
  }
}

template <int BM, int LDA, int LDB, int AUXA>
DEV void small_gemm(const int wv, const u16* A, const u16* B, f32x4 (&acc)[BM / 32][2], char* smem) {
  constexpr int MT = BM / 32, NLD = BM / 64 + 2, NS = (BM == 128) ? 4 : 5;
  const int tid = tid_of(wv);
  const int lane = tid & 63, wr = wv >> 2, wc = wv & 3, fr = lane & 15, fq = lane >> 4;
  const __amdgpu_buffer_rsrc_t rsa = __builtin_amdgcn_make_buffer_rsrc((void*)uni(A), 0, 0x7FFFFFFF, 0x00020000), rsb = __builtin_amdgcn_make_buffer_rsrc((void*)uni(B), 0, 0x7FFFFFFF, 0x00020000);
  u32 voa, vob;
  { int r, c; stage_rc(tid * 16, r, c); voa = (u32)(r * LDA + c) * 2u; const int b = tid * 16, rb = b >> 8; vob = (u32)(rb * LDB + ((((b >> 4) & 15) ^ swzb(rb)) * 8)) * 2u; }
  constexpr int STG = BM * 128 + 16384;
  auto stage = [&](int kt, int slot) {
    char* sa = smem + slot * STG + tid * 16;
    __builtin_amdgcn_raw_ptr_buffer_load_lds(rsa, (LDSP void*)sa, 16, voa, kt * 128, 0, AUXA);
    if (BM == 128) __builtin_amdgcn_raw_ptr_buffer_load_lds(rsa, (LDSP void*)(sa + 8192), 16, voa, kt * 128 + 64 * LDA * 2, 0, AUXA);
    __builtin_amdgcn_raw_ptr_buffer_load_lds(rsb, (LDSP void*)(sa + BM * 128), 16, vob, kt * 64 * LDB * 2, 0, 0);
    __builtin_amdgcn_raw_ptr_buffer_load_lds(rsb, (LDSP void*)(sa + BM * 128 + 8192), 16, vob, (kt * 64 + 32) * LDB * 2, 0, 0);
  };
  int bo[2];
#pragma unroll
  for (int t = 0; t < 2; ++t) { const int row = 8 * fq + 4 * t + (fr >> 2); bo[t] = BM * 128 + 256 * row + 16 * ((4 * wc + (fr & 3)) ^ swzb(row)); }
  const int ao = lds_byte(wr * (BM / 2) + fr, fq * 8);
#pragma unroll
  for (int m = 0; m < MT; ++m) acc[m][0] = acc[m][1] = f32x4{0, 0, 0, 0};
#pragma unroll
  for (int k = 0; k < NS - 1; ++k) stage(k, k);
  constexpr int nt = 16;
  int cur = 0, nxt = NS - 1;
  for (int kt = 0; kt < nt; ++kt) {
    if (kt + NS - 2 < nt) asm volatile("s_waitcnt vmcnt(%0)" ::"n"((NS - 2) * NLD) : "memory"); else asm volatile("s_waitcnt vmcnt(0)" ::: "memory");
    __builtin_amdgcn_s_barrier();
    if (kt + NS - 1 < nt) stage(kt + NS - 1, nxt);
    const char* sa = smem + cur * STG;
#pragma unroll
    for (int k = 0; k < 2; ++k) {
      const bf16x8 b0 = cat4(trread(sa + k * 8192 + bo[0]), trread(sa + k * 8192 + bo[1])), b1 = cat4(trread(sa + k * 8192 + bo[0] + 8), trread(sa + k * 8192 + bo[1] + 8));
#pragma unroll
      for (int m = 0; m < MT; ++m) {
        const bf16x8 a = *(const bf16x8*)(sa + ao + m * 2048 + k * 1024);
        acc[m][0] = mfma16(b0, a, acc[m][0]); acc[m][1] = mfma16(b1, a, acc[m][1]);
      }
    }
    asm volatile("s_waitcnt lgkmcnt(0)" ::: "memory");
    cur = cur == NS - 1 ? 0 : cur + 1; nxt = nxt == NS - 1 ? 0 : nxt + 1;
  }
}
template <int BM>
DEV void wo_item(const int wv, const Params& p, int g, int nt_, int kq, char* smem) {
  constexpr int MT = BM / 32;
  f32x4 acc[MT][2];
  small_gemm<BM, HD, D_, 16>(wv, (const u16*)(p.ws + WS_O) + (size_t)g * BM * HD + kq * 1024, p.Wo + (size_t)kq * 1024 * D_ + nt_ * 128, acc, smem);
  const int tid = tid_of(wv);
  const int lane = tid & 63, wr = wv >> 2, wc = wv & 3, fr = lane & 15, fq = lane >> 4;
  const int tile = (BM == 128 ? g : g - 15) * 8 + nt_;
  char* part = p.ws + WS_P + (size_t)tile * (4 * 65536);
  const __amdgpu_buffer_rsrc_t rp = __builtin_amdgcn_make_buffer_rsrc((void*)part, 0, 0x7FFFFFFF, 0x00020000);
#pragma unroll
  for (int m = 0; m < MT; ++m)
#pragma unroll
    for (int n = 0; n < 2; ++n) __builtin_amdgcn_raw_buffer_store_b128(__builtin_bit_cast(u32x4, acc[m][n]), rp, tid * 16 + (m * 2 + n) * 8192, kq * 65536, 16);
  asm volatile("s_waitcnt vmcnt(0)" ::: "memory");
  __syncthreads();
  volatile int* s_last = (volatile int*)(smem + 128 * 1024 + 32);
  if (tid == 0) *s_last = (int)ctl_add((u32*)(p.ws + WS_CTL) + C_P + tile, 1);
  __syncthreads();
  if (__builtin_amdgcn_readfirstlane(*s_last) != 3) return;
  if (wv == 0) {
    u32* cy = (u32*)(p.ws + WS_CTL) + C_Y + (g * BM >> 8) * 4 + (nt_ >> 1);
    for (int sp = 0; __builtin_amdgcn_readfirstlane(ctl_ld(cy)) < 2; ++sp) { __builtin_amdgcn_s_sleep(6); if (sp > 400000) { ctl_st((u32*)(p.ws + WS_CTL) + C_TMO, 1); break; } }
  }
  __syncthreads();
  const float* Y1 = (const float*)(p.ws + WS_Y); const float* Y2 = (const float*)(p.ws + WS_Y + (8u << 20));
  const __amdgpu_buffer_rsrc_t ry = __builtin_amdgcn_make_buffer_rsrc((void*)Y1, 0, 0x7FFFFFFF, 0x00020000);
#pragma unroll
  for (int m = 0; m < MT; ++m) {
    const u32 o = (u32)(g * BM + wr * (BM / 2) + m * 16 + fr) * D_ + nt_ * 128 + wc * 32 + fq * 8;
    f32x4 v[2];
#pragma unroll
    for (int n = 0; n < 2; ++n) {
      f32x4 t[4];
#pragma unroll
      for (int k = 0; k < 4; ++k) t[k] = __builtin_bit_cast(f32x4, __builtin_amdgcn_raw_buffer_load_b128(rp, tid * 16 + (m * 2 + n) * 8192, k * 65536, 16));
      const f32x4 y1 = __builtin_bit_cast(f32x4, __builtin_amdgcn_raw_buffer_load_b128(ry, o * 4 + n * 16, 0, 16)), y2 = __builtin_bit_cast(f32x4, __builtin_amdgcn_raw_buffer_load_b128(ry, o * 4 + n * 16, 8u << 20, 16));
      v[n] = ((t[0] + t[1]) + (t[2] + t[3])) + (y1 + y2);
    }
    *(u32x4*)(p.out + o) = u32x4{cvt_pk(v[0][0], v[0][1]), cvt_pk(v[0][2], v[0][3]), cvt_pk(v[1][0], v[1][1]), cvt_pk(v[1][2], v[1][3])};
  }
}

DEV void kv0_item(const int wv, const Params& p, int j, char* smem) {
  const int hf = j & 1, mat = (j >> 1) & 1, n0 = (j >> 2) * 128;
  f32x4 acc[4][2];
  small_gemm<128, D_, HD, 0>(wv, (const u16*)(p.ws + WS_XN) + (size_t)hf * 128 * D_, (mat ? p.Wv : p.Wk) + n0, acc, smem);
  const int tid = tid_of(wv), lane = tid & 63, wr = wv >> 2, wc = wv & 3, fr = lane & 15, fq = lane >> 4;
  const __amdgpu_buffer_rsrc_t rc = __builtin_amdgcn_make_buffer_rsrc((void*)((u16*)(p.ws + (mat ? WS_V : WS_K)) + (size_t)hf * 128 * HD + n0), 0, 0x7FFFFFFF, 0x00020000);
#pragma unroll
  for (int m = 0; m < 4; ++m)
    __builtin_amdgcn_raw_buffer_store_b128(u32x4{cvt_pk(acc[m][0][0], acc[m][0][1]), cvt_pk(acc[m][0][2], acc[m][0][3]), cvt_pk(acc[m][1][0], acc[m][1][1]), cvt_pk(acc[m][1][2], acc[m][1][3])},
                                           rc, ((wr * 64 + m * 16 + fr) * HD + wc * 32 + fq * 8) * 2, 0, 16);
}
constexpr int LDS_BYTES = 128 * 1024 + 64;
DEV void wait_cnts(const int wv, u32* ctl, int ia, int na, u32 ta, int ib, int nb, u32 tb) {
  if (wv == 0) {
    const int t = tid_of(0);
    u32* c = ctl + (t < na ? ia + t : (t < na + nb ? ib + t - na : ia));
    const u32 target = t < na ? ta : (t < na + nb ? tb : 0u);
    for (int sp = 0;; ++sp) {
      if (__all(ctl_ld(c) >= target)) break;
      __builtin_amdgcn_s_sleep(6);
      if (sp > 400000 || ((sp & 63) == 63 && __builtin_amdgcn_readfirstlane(ctl_ld(ctl + C_TMO)))) { ctl_st(ctl + C_TMO, 1); break; }
    }
  }
}
DEV void acquire_wg(const int wv) {
  if (tid_of(wv) == 0) { __builtin_amdgcn_fence(__ATOMIC_ACQUIRE, "agent"); asm volatile("s_waitcnt vmcnt(0)" ::: "memory"); }
  __syncthreads();
}
DEV void signal_wg(const int wv, u32* c) {
  asm volatile("s_waitcnt vmcnt(0)" ::: "memory");
  __syncthreads();
  if (tid_of(wv) == 0) ctl_add(c, 1);
}
#define ORD 1
#define NBIG 4
constexpr int N_KV0 = 128, N_KV = N_KV0 + 224, N_F1 = 112, N_Q = 128, N_Y = 64, N_G = 16 * 48, I_Q = N_KV, I_F1 = I_Q + N_Q, I_Y = I_F1 + N_F1, I_G = I_Y + N_Y, N_ITEMS = I_G + N_G;
typedef __attribute__((address_space(4))) const Params* KArg;
DEV void worker(const int wv, const int role, char* smem) {
  volatile int* s_idx = (volatile int*)(smem + 128 * 1024 + 16);
  int qn = 4 + ((role >> 5) & 3), tried = 0;
  for (;;) {
    KArg ka = (KArg)__builtin_amdgcn_kernarg_segment_ptr(); asm volatile("" : "+s"(ka));
    Params p; p.x = ka->x; p.mem0 = ka->mem0; p.Wq = ka->Wq; p.Wk = ka->Wk; p.Wv = ka->Wv; p.Wo = ka->Wo; p.w1 = ka->w1; p.b1 = ka->b1; p.w2 = ka->w2; p.out = ka->out; p.ws = ka->ws;
    u32* ctl = (u32*)(p.ws + WS_CTL);
    const u16* XN = (const u16*)(p.ws + WS_XN);
    __syncthreads();
    if (tid_of(wv) == 0) *s_idx = (int)ctl_add(ctl + C_QUEUE + qn * 32, 1);
    __syncthreads();
    int idx = __builtin_amdgcn_readfirstlane(*s_idx);
    if (qn >= 4) {
      const int x = qn - 4, nf = 8 * (x < 2 ? 4 : 3);
      if (idx >= 88 + nf + 32 + 16) { if (++tried == 4) { tried = 0; qn = (role >> 5) & 3; } else qn = 4 + ((x + 1) & 3); continue; }
      auto kv = [&](int m) { const int r = m & 7; return N_KV0 + (m >> 3) * 32 + (r >> 2) * 16 + (r & 3) * 4 + x; };
      auto f1 = [&](int m) { const int per = nf >> 3, mt = m / per; return I_F1 + mt * 14 + (m - mt * per) * 4 + x; };
      auto qq = [&](int m) { return I_Q + (m >> 2) * 16 + (m & 3) * 4 + x; };
      auto yy = [&](int m) { return I_Y + ((m >> 1) * 4 + x) * 2 + (m & 1); };
      if (idx < 32) idx = ((idx >> 2) * 4 + x) * 4 + (idx & 3);
      else if (idx < 64) idx = kv(idx - 32);
      else if (idx < 64 + nf) idx = f1(idx - 64);
      else if (idx < 88 + nf) idx = kv(idx - nf - 32);
      else if (ORD == 0) { if (idx < 104 + nf) idx = yy(idx - 88 - nf); else idx = qq(idx - 104 - nf); }
      else if (ORD == 1) { if (idx < 104 + nf) idx = qq(idx - 88 - nf); else if (idx < 120 + nf) idx = yy(idx - 104 - nf); else idx = qq(idx - 104 - nf); }
      else { if (idx < 120 + nf) idx = qq(idx - 88 - nf); else idx = yy(idx - 120 - nf); }
    }
    else {
      if (idx >= 200) { if (++tried == 4) break; qn = (qn + 1) & 3; continue; }
      int g, k;
      if (idx < 4) { g = 0; k = idx; } else { const int u = idx - 4, gg = u / 12, kk = u - gg * 12;
        if (gg < 15) { if (kk < 4) { g = gg + 1; k = kk; } else { g = gg; k = kk; } } else { g = 15; k = u - 180 + 4; } }
      if (g < 2 * NBIG && k >= 4) {
        if (!(g & 1) || k > 5) continue;
        idx = I_G + N_G + 64 + ((g >> 1) * 4 + qn) * 2 + (k - 4);
      } else
      idx = (g == 15 && k >= 4) ? I_G + N_G + qn * 16 + (k - 4) : I_G + g * 48 + (k < 4 ? k * 4 + qn : 16 + qn * 8 + (k - 4));
    }
    if (idx < N_KV0) {
      kv0_item(wv, p, idx, smem);
      signal_wg(wv, ctl + C_KV);
    } else if (idx < I_G || idx >= I_G + N_G + 64) {
      const u16 *A, *B, *bias; void* C; int lda, ldb, K, ldc, mode; u32* done; char* pbuf = p.ws; u32* pair = ctl;
      if (idx < I_Q) { const int jj = idx - N_KV0 + 32, mt = jj >> 5, mat = (jj >> 4) & 1, n0 = (jj & 15) * 256;
        A = XN + (size_t)mt * 256 * D_; lda = D_; B = (mat ? p.Wv : p.Wk) + n0; ldb = HD; K = D_; C = (u16*)(p.ws + (mat ? WS_V : WS_K)) + (size_t)mt * 256 * HD + n0; ldc = HD; bias = p.b1; mode = 0; done = ctl + C_KV + mt * 32;
      } else if (idx >= I_F1 && idx < I_Y) { const int j = idx - I_F1, mt = j / 14, n0 = (j - mt * 14) * 256;
        A = XN + (size_t)mt * 256 * D_; lda = D_; B = p.w1 + n0; ldb = FF; K = D_; C = (u16*)(p.ws + WS_ACT) + (size_t)mt * 256 * FF + n0; ldc = FF; bias = p.b1 + n0; mode = 1; done = ctl + C_F1 + mt * 32;
      } else if (idx < I_F1) { const int j = idx - I_Q, mt = j >> 4, n0 = (j & 15) * 256;
        A = XN + (size_t)mt * 256 * D_; lda = D_; B = p.Wq + n0; ldb = HD; K = D_; C = (u16*)(p.ws + WS_Q) + (size_t)mt * 256 * HD + n0; ldc = HD; bias = p.b1; mode = 0; done = ctl + C_Q + j;
      } else if (idx >= I_G) { const int j = idx - (I_G + N_G + 64), kh = j & 1, mt = j >> 3, ct = (j >> 1) & 3, n0 = ct * 256;
        wait_cnts(wv, ctl, C_A + mt * 8 + kh * 2, 2, 4, C_A + mt * 8 + 4 + kh * 2, 2, 4);
        if (kh) wait_cnts(wv, ctl, C_Y + mt * 4 + ct, 1, 2, 0, 0, 0);
        acquire_wg(wv);
        A = (const u16*)(p.ws + WS_O) + (size_t)mt * 256 * HD + kh * 2048; lda = HD; B = p.Wo + (size_t)kh * 2048 * D_ + n0; ldb = D_; K = 2048; C = p.out + (size_t)mt * 256 * D_ + n0; ldc = D_;
        bias = (const u16*)((const float*)(p.ws + WS_Y) + (size_t)mt * 256 * D_ + n0); mode = 4 + kh; done = ctl + C_TMO + 1;
        pbuf = p.ws + WS_P + (size_t)(mt * 16 + ct * 2) * 262144; pair = ctl + C_P + mt * 16 + ct;
      } else { const int j = idx - I_Y, kh = j & 1, mt = j >> 3, n0 = ((j >> 1) & 3) * 256;
        wait_cnts(wv, ctl, C_F1 + mt * 32, 1, 14, 0, 0, 0); acquire_wg(wv);
        A = (const u16*)(p.ws + WS_ACT) + (size_t)mt * 256 * FF + kh * 1792; lda = FF; B = p.w2 + (size_t)kh * 1792 * D_ + n0; ldb = D_; K = 1792;
        C = (float*)(p.ws + WS_Y + (size_t)kh * (8u << 20)) + (size_t)mt * 256 * D_ + n0; ldc = D_; bias = p.x + (size_t)mt * 256 * D_ + n0; mode = kh ? 3 : 2; done = ctl + C_Y + (j >> 1);
      }
      const int dn = __builtin_amdgcn_readfirstlane((int)(done - ctl));
      gemm256(wv, __builtin_amdgcn_readfirstlane(mode), A, __builtin_amdgcn_readfirstlane(lda), B, __builtin_amdgcn_readfirstlane(ldb), __builtin_amdgcn_readfirstlane(K), C, __builtin_amdgcn_readfirstlane(ldc), uni(bias), pbuf, uni(pair), smem);
      KArg kb = (KArg)__builtin_amdgcn_kernarg_segment_ptr(); asm volatile("" : "+s"(kb));
      signal_wg(wv, (u32*)(kb->ws + WS_CTL) + dn);
    } else if (idx >= I_G + N_G) {
      const int j = idx - I_G - N_G, kq = j >> 4, nt_ = j & 7, hf = (j >> 3) & 1;
      wait_cnts(wv, ctl, C_A + 60 + kq, 1, 4, 0, 0, 0);
      __syncthreads();
      wo_item<64>(wv, p, 30 + hf, nt_, kq, smem);
    } else {
      const int j = idx - I_G, g = j / 48, k = j - g * 48;
      if (k < 16) {
        const int seg = 4 * g + (k >> 2), h0 = 8 * (k & 3);
        wait_cnts(wv, ctl, C_Q + (seg >> 3) * 16 + (h0 >> 1), 4, 1, C_KV + (seg >> 3) * 32, 1, seg < 8 ? 128 : 32);
        __syncthreads();
        attn_item(wv, p, seg, h0 + wv, smem + wv * 12288);
        signal_wg(wv, ctl + C_A + g * 4 + (k & 3));
      } else {
        const int kq = (k - 16) >> 3, nt_ = (k - 16) & 7;
        wait_cnts(wv, ctl, C_A + g * 4 + kq, 1, 4, 0, 0, 0);
        __syncthreads();
        wo_item<128>(wv, p, g, nt_, kq, smem);
      }
    }
  }
}
__global__ void __launch_bounds__(512, 2) k_main(Params p) {
  extern __shared__ __attribute__((aligned(16))) char smem[];
  const int wv = __builtin_amdgcn_readfirstlane(threadIdx.x >> 6);
  const int role = take_role((u32*)(p.ws + WS_CTL), (int*)(smem + 128 * 1024));
  if (role >= 0 && role < 128) chain_role(p, role, smem);
  __syncthreads();
  worker(wv, role, smem);
}
extern "C" void kernel_launch(void* const* d_in, const int* in_sizes, int n_in, void* d_out, int out_size, void* d_ws, size_t ws_size, hipStream_t stream) {
  Params p;
  p.x = (const u16*)d_in[0]; p.mem0 = (const u16*)d_in[1]; p.Wq = (const u16*)d_in[2]; p.Wk = (const u16*)d_in[3]; p.Wv = (const u16*)d_in[4]; p.Wo = (const u16*)d_in[5];
  p.w1 = (const u16*)d_in[8]; p.b1 = (const u16*)d_in[9]; p.w2 = (const u16*)d_in[10]; p.out = (u16*)d_out; p.ws = (char*)d_ws;
  (void)hipFuncSetAttribute((const void*)k_main, hipFuncAttributeMaxDynamicSharedMemorySize, LDS_BYTES);
  k_prep<<<512, 256, 0, stream>>>(p);
  k_main<<<256, 512, LDS_BYTES, stream>>>(p);
}
```

```cpp
#include <hip/hip_runtime.h>
#include <hip/hip_bf16.h>
#include <stdint.h>

typedef unsigned int u32;
typedef unsigned short u16;
typedef u32 u32x4 __attribute__((ext_vector_type(4)));
typedef u32 u32x2 __attribute__((ext_vector_type(2)));
typedef short bf16x8 __attribute__((ext_vector_type(8)));
typedef short s16x4 __attribute__((ext_vector_type(4)));
typedef float f32x4 __attribute__((ext_vector_type(4)));
typedef __bf16 bf2_t __attribute__((ext_vector_type(2)));
typedef float f32x2 __attribute__((ext_vector_type(2)));
#define LDSP __attribute__((address_space(3)))
#define DEV __device__ __forceinline__

constexpr int S_ = 2048, D_ = 1024, HD = 4096, FF = 3584, NSEG = 64;
constexpr u32 SENT = 0xFFFFFFFFu;
constexpr size_t WS_XN = 0;
constexpr size_t WS_Q = 4u << 20;
constexpr size_t WS_K = 20u << 20;
constexpr size_t WS_V = 36u << 20;
constexpr size_t WS_ACT = 52u << 20;
constexpr size_t WS_O = 68u << 20;
constexpr size_t WS_KM = 84u << 20;
constexpr size_t WS_VM = 88u << 20;
constexpr size_t WS_CTL = 92u << 20;
constexpr size_t WS_XCH = 96u << 20;
constexpr size_t WS_Y = 100u << 20;
constexpr size_t WS_P = 116u << 20;
constexpr u32 SLOT = 256 * 1024, X_M = 0, X_O = 32768, X_SC = 98304;
constexpr int C_TICK = 0;
constexpr int C_QUEUE = 1408  , C_F1 = 512, C_Q = C_F1 + 8 * 32, C_Y = C_Q + 128, C_A = C_Y + 32  , C_P = C_A + 128  , C_KV = 1600  , C_KM = 2048;
constexpr int C_OVF = 8 * 32, C_TOTAL = 9 * 32, C_NEAR = 10 * 32, C_TMO = 11 * 32;

DEV u32 cvt_pk(float a, float b) { f32x2 v = {a, b}; bf2_t r = __builtin_convertvector(v, bf2_t); return __builtin_bit_cast(u32, r); }
DEV float bf_lo(u32 x) { return __uint_as_float(x << 16); }
DEV float bf_hi(u32 x) { return __uint_as_float(x & 0xFFFF0000u); }
DEV u32 nosent(u32 x) { return x == SENT ? 0xFFFEFFFFu : x; }
DEV float ror8(float x) { return __int_as_float(__builtin_amdgcn_update_dpp(0, __float_as_int(x), 0x128, 0xF, 0xF, true)); }
DEV u32 ror8u(u32 x) { return (u32)__builtin_amdgcn_update_dpp(0, (int)x, 0x128, 0xF, 0xF, true); }
DEV float xor16(float x) { u32 a = __float_as_uint(x); auto r = __builtin_amdgcn_permlane16_swap(a, a, false, false); return __uint_as_float((threadIdx.x & 16) ? r[0] : r[1]); }
DEV float xor32(float x) { u32 a = __float_as_uint(x); auto r = __builtin_amdgcn_permlane32_swap(a, a, false, false); return __uint_as_float((threadIdx.x & 32) ? r[0] : r[1]); }
DEV f32x4 mfma16(bf16x8 a, bf16x8 b, f32x4 c) { return __builtin_amdgcn_mfma_f32_16x16x32_bf16(a, b, c, 0, 0, 0); }
DEV bf16x8 asfrag(u32x4 v) { return __builtin_bit_cast(bf16x8, v); }
DEV s16x4 trread(const char* p) { return __builtin_amdgcn_ds_read_tr16_b64_v4i16((LDSP s16x4*)p); }
DEV bf16x8 cat4(s16x4 a, s16x4 b) { return __builtin_shufflevector(a, b, 0, 1, 2, 3, 4, 5, 6, 7); }
DEV bool valid4(u32x4 v) { return (v.x != SENT) & (v.y != SENT) & (v.z != SENT) & (v.w != SENT); }
DEV u32x4 ld_sc1(__amdgpu_buffer_rsrc_t rs, u32 off) { return __builtin_amdgcn_raw_buffer_load_b128(rs, off, 0, 16); }
DEV void st_sc1(__amdgpu_buffer_rsrc_t rs, u32 off, u32x4 v) { __builtin_amdgcn_raw_buffer_store_b128(v, rs, off, 0, 16); }
DEV u32 ctl_ld(u32* p) { return __hip_atomic_load(p, __ATOMIC_RELAXED, __HIP_MEMORY_SCOPE_AGENT); }
DEV void ctl_st(u32* p, u32 v) { __hip_atomic_store(p, v, __ATOMIC_RELAXED, __HIP_MEMORY_SCOPE_AGENT); }
DEV u32 ctl_add(u32* p, u32 v) { return __hip_atomic_fetch_add(p, v, __ATOMIC_RELAXED, __HIP_MEMORY_SCOPE_AGENT); }
DEV char* xch_base(char* ws) { return (char*)(((size_t)(ws + WS_XCH) + (2u << 20) - 1) & ~(size_t)((2u << 20) - 1)); }

DEV int tid_of(int wv) { int ln; asm volatile("v_mbcnt_lo_u32_b32 %0, -1, 0\n v_mbcnt_hi_u32_b32 %0, -1, %0" : "=v"(ln)); return wv * 64 + ln; }
template <class T> DEV T* uni(T* p) {
  const unsigned long long a = (unsigned long long)p;
  return (T*)(((unsigned long long)(u32)__builtin_amdgcn_readfirstlane((int)(a >> 32)) << 32) | (u32)__builtin_amdgcn_readfirstlane((int)a));
}

struct Params {
  const u16* x; const u16* mem0; const u16* Wq; const u16* Wk; const u16* Wv; const u16* Wo;
  const u16* w1; const u16* b1; const u16* w2; u16* out; char* ws;
};

__global__ void __launch_bounds__(256) k_prep(Params p) {
  const int tid = threadIdx.x, lane = tid & 63, wave = tid >> 6, b = blockIdx.x;
  {
    const int row = b * 4 + wave;
    const u32x4* src = (const u32x4*)(p.x + (size_t)row * D_);
    u32x4 v0 = src[lane], v1 = src[lane + 64];
    float f[16];
    f[0] = bf_lo(v0.x); f[1] = bf_hi(v0.x); f[2] = bf_lo(v0.y); f[3] = bf_hi(v0.y); f[4] = bf_lo(v0.z); f[5] = bf_hi(v0.z); f[6] = bf_lo(v0.w); f[7] = bf_hi(v0.w);
    f[8] = bf_lo(v1.x); f[9] = bf_hi(v1.x); f[10] = bf_lo(v1.y); f[11] = bf_hi(v1.y); f[12] = bf_lo(v1.z); f[13] = bf_hi(v1.z); f[14] = bf_lo(v1.w); f[15] = bf_hi(v1.w);
    float s = 0; for (int i = 0; i < 16; ++i) s += f[i];
    for (int o = 32; o; o >>= 1) s += __shfl_xor(s, o);
    const float mean = s * (1.f / D_);
    float q = 0; for (int i = 0; i < 16; ++i) { f[i] -= mean; q += f[i] * f[i]; }
    for (int o = 32; o; o >>= 1) q += __shfl_xor(q, o);
    const float rs = 1.f / sqrtf(q * (1.f / D_) + 1e-5f);
    u32x4 o0 = {cvt_pk(f[0] * rs, f[1] * rs), cvt_pk(f[2] * rs, f[3] * rs), cvt_pk(f[4] * rs, f[5] * rs), cvt_pk(f[6] * rs, f[7] * rs)};
    u32x4 o1 = {cvt_pk(f[8] * rs, f[9] * rs), cvt_pk(f[10] * rs, f[11] * rs), cvt_pk(f[12] * rs, f[13] * rs), cvt_pk(f[14] * rs, f[15] * rs)};
    u32x4* dst = (u32x4*)(p.ws + WS_XN + (size_t)row * D_ * 2);
    dst[lane] = o0; dst[lane + 64] = o1;
  }
  {
    const u32 off = (u32)(b * 256 + tid) * 16;
    const u32 x = ((off >> 14) << 13) | (off & 8191);
    u32x4 v = {SENT, SENT, SENT, SENT};
    if (x < 32768) {
      const u32 c8 = x >> 8, g = (x >> 7) & 1, r = (x >> 4) & 7;
      if (g == 0) { v = *(const u32x4*)(p.mem0 + r * D_ + c8 * 8); v.x = nosent(v.x); v.y = nosent(v.y); v.z = nosent(v.z); v.w = nosent(v.w); }
      else v = u32x4{0, 0, 0, 0};
    }
    *(u32x4*)(xch_base(p.ws) + off) = v;
  }
  if (b == 0) { u32* ctl = (u32*)(p.ws + WS_CTL); for (int i = tid; i < 4096; i += 256) ctl[i] = 0; }
}

DEV int take_role(u32* ctl, int* s_tmp) {
  if (threadIdx.x == 0) {
    u32 xcc; asm volatile("s_getreg_b32 %0, hwreg(HW_REG_XCC_ID)" : "=s"(xcc)); xcc &= 7;
    const u32 t = ctl_add(ctl + C_TICK + xcc * 32, 1);
    int role;
    if (t < 32) { role = xcc * 32 + t; ctl_add(ctl + C_TOTAL, 1); }
    else {
      const u32 k = ctl_add(ctl + C_OVF, 1); ctl_add(ctl + C_TOTAL, 1);
      int sp = 0; while (ctl_ld(ctl + C_TOTAL) < gridDim.x && ++sp < 2000000) __builtin_amdgcn_s_sleep(2);
      role = -1; u32 n = 0;
      for (int xx = 0; xx < 8; ++xx) { const u32 c = ctl_ld(ctl + C_TICK + xx * 32); if (c < 32) { if (k < n + (32 - c)) { role = xx * 32 + c + (k - n); break; } n += 32 - c; } }
    }
    *s_tmp = role;
  }
  __syncthreads();
  return __builtin_amdgcn_readfirstlane(*s_tmp);
}
DEV float red16_add(float x) { u32 a = __float_as_uint(x); auto r = __builtin_amdgcn_permlane16_swap(a, a, false, false); return __uint_as_float(r[0]) + __uint_as_float(r[1]); }
DEV float red32_add(float x) { u32 a = __float_as_uint(x); auto r = __builtin_amdgcn_permlane32_swap(a, a, false, false); return __uint_as_float(r[0]) + __uint_as_float(r[1]); }
DEV float red16_max(float x) { u32 a = __float_as_uint(x); auto r = __builtin_amdgcn_permlane16_swap(a, a, false, false); return fmaxf(__uint_as_float(r[0]), __uint_as_float(r[1])); }
DEV float red32_max(float x) { u32 a = __float_as_uint(x); auto r = __builtin_amdgcn_permlane32_swap(a, a, false, false); return fmaxf(__uint_as_float(r[0]), __uint_as_float(r[1])); }
DEV f32x4 halfsum4(f32x4 a, f32x4 b) {
  float d0, d1, d2, d3;
  asm volatile("s_nop 7\n s_nop 3\n"
               "v_add_f32_dpp %0, %4, %4 row_ror:8 row_mask:0xf bank_mask:0x3\n v_add_f32_dpp %1, %5, %5 row_ror:8 row_mask:0xf bank_mask:0x3\n"
               "v_add_f32_dpp %2, %6, %6 row_ror:8 row_mask:0xf bank_mask:0x3\n v_add_f32_dpp %3, %7, %7 row_ror:8 row_mask:0xf bank_mask:0x3\n"
               "v_add_f32_dpp %0, %8, %8 row_ror:8 row_mask:0xf bank_mask:0xc\n v_add_f32_dpp %1, %9, %9 row_ror:8 row_mask:0xf bank_mask:0xc\n"
               "v_add_f32_dpp %2, %10, %10 row_ror:8 row_mask:0xf bank_mask:0xc\n v_add_f32_dpp %3, %11, %11 row_ror:8 row_mask:0xf bank_mask:0xc\n s_nop 1"
               : "=&v"(d0), "=&v"(d1), "=&v"(d2), "=&v"(d3)
               : "v"(a[0]), "v"(a[1]), "v"(a[2]), "v"(a[3]), "v"(b[0]), "v"(b[1]), "v"(b[2]), "v"(b[3]));
  return f32x4{d0, d1, d2, d3};
}
DEV u32 nos(u32 x) { return min(x, 0xFFFFFFFEu); }
#define BARL() asm volatile("s_waitcnt lgkmcnt(0)\n s_barrier" ::: "memory")
DEV void nap(int n) { for (int i = 0; i < n; ++i) __builtin_amdgcn_s_sleep(1); }
#define CH_D1 12
#define CH_D3 10
#define CH_D2 8
#define SMW 2
#define CH_INC 1
constexpr int SPIN_CAP = 150000;
constexpr int L_RQK = 0;
constexpr int L_RV = 8 * 584 * 4;
constexpr int L_R2 = L_RV + 8 * 296 * 4;
constexpr int L_KT = L_R2 + 8 * 160 * 4;
constexpr int L_VT = L_KT + 2 * 32 * 64;
constexpr int L_VM = L_VT + 2 * 32 * 64;
constexpr int L_F = L_VM + 16 * 64 + 64;
constexpr int L_W2 = L_F + 1024;
constexpr int L_CHAIN_END = L_VM + 16 * 64;

#define POLL(N, V, LDEXPR, NSP) do { \
    if (dead) { _Pragma("unroll") for (int k = 0; k < N; ++k) V[k] = u32x4{0, 0, 0, 0}; } \
    else for (int sp = 0;; ++sp) { \
      asm volatile("" ::: "memory"); \
      _Pragma("unroll") for (int k = 0; k < N; ++k) V[k] = (LDEXPR); \
      u32 mxv = 0; \
      _Pragma("unroll") for (int k = 0; k < N; ++k) mxv = max(max(mxv, max(V[k].x, V[k].y)), max(V[k].z, V[k].w)); \
      if (__all(mxv != SENT)) { NSP += sp; break; } \
      if (sp > SPIN_CAP || ((sp & 255) == 255 && __builtin_amdgcn_readfirstlane(ctl_ld(ctl + C_TMO)))) { ctl_st(ctl + C_TMO, 1); dead = true; break; } \
    } } while (0)
DEV u32x4 ldx(__amdgpu_buffer_rsrc_t rs, u32 voff, u32 soff) { return __builtin_amdgcn_raw_buffer_load_b128(rs, voff, soff, 16); }
DEV void stx(__amdgpu_buffer_rsrc_t rs, u32 voff, u32 soff, u32x4 v) { __builtin_amdgcn_raw_buffer_store_b128(v, rs, voff, soff, 16); }

DEV void chain_role(const Params& p, const int role, char* smem) {
  const int tid = threadIdx.x, w = __builtin_amdgcn_readfirstlane(tid >> 6), l = tid & 63, q = l >> 4, i = l & 15, r = l & 7, g2 = (l >> 3) & 1;
  const int h = role >> 2, j = role & 3, cs = role >> 1, g = role & 1;
  u32* ctl = (u32*)(p.ws + WS_CTL);
  char* xb = xch_base(p.ws);
  if (role == 0 && w == 0) {
    u32 lat[2] = {1u << 30, 1u << 30};
    for (int rep = 0; rep < 24; ++rep) {
      const int c = rep & 1;
      u32* pq = (u32*)(xb + 127 * 16384 + c * 8192 + 8064) + (l & 31);
      const long long t0 = clock64();
      asm volatile("global_store_dword %0, %1, off sc1\n s_waitcnt vmcnt(0)" ::"v"(pq), "v"(SENT) : "memory");
      const u32 dt = (u32)(clock64() - t0);
      if (c) lat[1] = min(lat[1], dt); else lat[0] = min(lat[0], dt);
    }
    if (l == 0) { ctl[C_NEAR + 1] = lat[0]; ctl[C_NEAR + 2] = lat[1]; ctl_st(ctl + C_NEAR, lat[1] < lat[0] ? 2u : 1u); }
  }
  bf16x8 W1[6][4];
  bf16x8 W2t[8];
  {
    char* stg = smem + w * 8192;
    const u16* mats[3] = {p.Wq, p.Wk, p.Wv};
    u32x4 raw[4][8];
#pragma unroll
    for (int m = 0; m < 3; ++m) {
      const u16* src = mats[m] + (size_t)(128 * w) * HD + h * 128 + 32 * j;
#pragma unroll
      for (int it = 0; it < 8; ++it) raw[m][it] = *(const u32x4*)(src + (size_t)(it * 16 + (l >> 2)) * HD + (l & 3) * 8);
    }
    {
      const u16* src = p.Wo + (size_t)(2048 * g + 256 * w) * D_ + 16 * cs;
#pragma unroll
      for (int it = 0; it < 8; ++it) raw[3][it] = *(const u32x4*)(src + (size_t)(it * 32 + (l >> 1)) * D_ + (l & 1) * 8);
    }
    __builtin_amdgcn_sched_barrier(0);
#pragma unroll
    for (int m = 0; m < 3; ++m) {
#pragma unroll
      for (int it = 0; it < 8; ++it) *(u32x4*)(stg + (it * 16 + (l >> 2)) * 64 + (l & 3) * 16) = raw[m][it];
      asm volatile("s_waitcnt lgkmcnt(0)" ::: "memory");
#pragma unroll
      for (int t = 0; t < 2; ++t)
#pragma unroll
        for (int ks = 0; ks < 4; ++ks) {
          const char* a = stg + (32 * ks + 8 * q + (i >> 2)) * 64 + t * 32 + (i & 3) * 8;
          W1[m * 2 + t][ks] = cat4(trread(a), trread(a + 4 * 64));
        }
      asm volatile("s_waitcnt lgkmcnt(0)" ::: "memory");
    }
#pragma unroll
    for (int it = 0; it < 8; ++it) *(u32x4*)(stg + (it * 32 + (l >> 1)) * 32 + (l & 1) * 16) = raw[3][it];
    asm volatile("s_waitcnt lgkmcnt(0)" ::: "memory");
#pragma unroll
    for (int ks = 0; ks < 8; ++ks) { const char* a = stg + (32 * ks + 8 * q + (i >> 2)) * 32 + (i & 3) * 8; W2t[ks] = cat4(trread(a), trread(a + 4 * 32)); }
    asm volatile("s_waitcnt lgkmcnt(0)" ::: "memory");
  }
  __syncthreads();
#pragma unroll
  for (int ks = 0; ks < 8; ++ks) *(bf16x8*)(smem + L_W2 + (w * 8 + ks) * 1024 + l * 16) = W2t[ks];
  const unsigned long long kvb = (unsigned long long)uni((const u16*)(p.ws + WS_K) + h * 128 + 32 * j);
  const u32x4 rkv = {(u32)__builtin_amdgcn_readfirstlane((int)kvb), (u32)__builtin_amdgcn_readfirstlane((int)(kvb >> 32)), 0x7FFFFFFFu, 0x00020000u};
  const u32 vkv = (u32)((l >> 2) * HD + (l & 3) * 8) * 2u;
  const u32 ldsk = (u32)(size_t)(LDSP char*)(smem + L_KT);
  auto dma = [&](u32 dst, u32 so) { asm volatile("s_mov_b32 m0, %0\n s_nop 0\n buffer_load_dwordx4 %1, %2, %3 offen sc1 lds" ::"s"(dst), "v"(vkv), "s"(rkv), "s"(so) : "memory"); };
  auto prefetch = [&](int s) {
    if ((s & 7) == 0) {
      u32* c = ctl + C_KV + (s >> 3) * 32;
      for (int sp = 0; __builtin_amdgcn_readfirstlane(ctl_ld(c)) < (s ? 32u : 128u); ++sp) { __builtin_amdgcn_s_sleep(4); if (sp > 1000000) { ctl_st(ctl + C_TMO, 1); break; } }
    }
    const u32 d = ldsk + (s & 1) * 2048, so = (u32)s * 32 * HD * 2;
    dma(d, so); dma(d + 1024, so + 16 * HD * 2); dma(d + 4096, so + (u32)(WS_V - WS_K)); dma(d + 4096 + 1024, so + (u32)(WS_V - WS_K) + 16 * HD * 2);
  };
  if (w == 7) { prefetch(0); asm volatile("s_waitcnt vmcnt(0)" ::: "memory"); }
  if (w == 0 && l < 32) *(u32x4*)(smem + L_VM + 512 + l * 16) = u32x4{0, 0, 0, 0};
  if (tid == 0) { int sp = 0; u32 c; while ((c = ctl_ld(ctl + C_NEAR)) == 0 && ++sp < 4000000) __builtin_amdgcn_s_sleep(1); *(volatile u32*)(smem + L_CHAIN_END) = c; }
  __syncthreads();
  const u32 near = (__builtin_amdgcn_readfirstlane(*(volatile u32*)(smem + L_CHAIN_END)) == 2) ? 8192u : 0u;
  const __amdgpu_buffer_rsrc_t rs = __builtin_amdgcn_make_buffer_rsrc((void*)(xb + near), 0, 0x7FFFFFFF, 0x00020000);
  auto PH = [](u32 x) -> u32 { return ((x >> 13) << 14) | (x & 8191); };
  constexpr u32 X_S0 = X_SC + 131072;
  const u32 oM = PH(X_M + 4 * w * 1024 + l * 16), oO = PH(X_O + g * 32768 + (32 * w + 4 * g2 + q) * 128 + r * 16);
  const u32 oS = PH(X_SC + h * 4096 + g2 * 512 + q * 128 + r * 16);
  const u32 oS0 = PH(X_S0 + h * 1024 + ((q >> 1) * 2 + g2) * 256 + (q & 1) * 128 + r * 16);
  const u32 pS0 = PH(X_S0 + h * 1024 + j * 256 + (q & 1) * 128 + r * 16);
  const u32 pO = PH(X_O + role * 512 + q * 128 + r * 16);
  const u32 pM = PH(X_M + (2 * cs + ((l >> 2) & 1)) * 256 + g * 128 + (l >> 3) * 16);
  const u32 jo = j * 1024;
  float* RQK = (float*)(smem + L_RQK); float* RV = (float*)(smem + L_RV); float* R2 = (float*)(smem + L_R2);
  const __amdgpu_buffer_rsrc_t rkm = __builtin_amdgcn_make_buffer_rsrc((void*)((u16*)(p.ws + WS_KM) + h * 128 + 32 * j), 0, 0x7FFFFFFF, 0x00020000);
  bool dead = false; int ns1 = 0, ns2 = 0, ns3 = 0;
  int d1 = (w == 1 ? CH_D1 - 4 : CH_D1), d2 = CH_D2, d3 = CH_D3;
#define TS(k)
  const u32x4 SV = {SENT, SENT, SENT, SENT};

  auto reset = [&](int s) {
    const u32 so = (u32)((s - 2) & 3) * (2 * SLOT);
    stx(rs, oS, so + jo, SV); if (l < 32 && g2 == 0) stx(rs, pS0, so, SV);
    if (g2 == 0) stx(rs, pO, so, SV);
    if ((l & 3) == 0) stx(rs, pM, so, SV);
  };
#define RESET() do { if (w == 6 && s >= 2) reset(s); } while (0)
#define PFPOS 3
#define RSPOS 0
  for (int s = 0; s < NSEG; ++s) {
    const u32 sb = (u32)(s & 3) * (2 * SLOT);
    u32x4 mv[4];
    if (s) nap(d1);
    TS(0);
    f32x4 acc[6];
    const f32x4 z4 = {0, 0, 0, 0};
    {
#pragma unroll
      for (int k = 0; k < 4; ++k) mv[k] = ldx(rs, oM + k * 1024, sb);
      if (PFPOS == 4 && w == 7 && s + 1 < NSEG) prefetch(s + 1);
#pragma unroll
      for (int ks = 0; ks < 4; ++ks)
#pragma unroll
        for (int t = 0; t < 4; ++t) acc[t] = mfma16(W1[t][ks], asfrag(mv[ks]), ks ? acc[t] : z4);
      u32 mxv = 0;
#pragma unroll
      for (int k = 0; k < 4; ++k) mxv = max(max(mxv, max(mv[k].x, mv[k].y)), max(mv[k].z, mv[k].w));
      if (!__all(mxv != SENT)) {
        ++ns1; d1 = min(d1 + CH_INC, CH_D1 + 10);
        POLL(4, mv, ldx(rs, oM + k * 1024, sb), ns1);
#pragma unroll
        for (int ks = 0; ks < 4; ++ks)
#pragma unroll
          for (int t = 0; t < 4; ++t) acc[t] = mfma16(W1[t][ks], asfrag(mv[ks]), ks ? acc[t] : z4);
      }
    }
    TS(1);
    {
      f32x4 a, b;
      a = halfsum4(acc[0], acc[2]); b = halfsum4(acc[1], acc[3]);
      float* d = RQK + w * 584 + g2 * 288 + r * 36 + 4 * q;
      *(f32x4*)d = a; *(f32x4*)(d + 16) = b;
    }
    BARL();
    TS(2);
    if (PFPOS == 0 && w == 7 && s + 1 < NSEG) prefetch(s + 1);
    if (RSPOS == 0) RESET();
    {
      const float* b = RQK + (l >> 5) * 288 + w * 36 + (l & 31);
      float v[8];
#pragma unroll
      for (int ww = 0; ww < 8; ++ww) v[ww] = b[ww * 584];
      __builtin_amdgcn_sched_barrier(0);
      const float t = ((v[0] + v[1]) + (v[2] + v[3])) + ((v[4] + v[5]) + (v[6] + v[7]));
      const float o = __int_as_float(__builtin_amdgcn_update_dpp(0, __float_as_int(t), 0xB1, 0xF, 0xF, true));
      if ((l & 1) == 0) *(u32*)(smem + L_F + (l >> 5) * 512 + w * 64 + (l & 31) * 2) = cvt_pk(t, o);
    }
    BARL();
    if (PFPOS == 3 && w == 7 && s + 1 < NSEG) prefetch(s + 1);
    if (RSPOS == 3) RESET();
    if (w == 0) {
      const u32x4 qf = *(const u32x4*)(smem + L_F + r * 64 + q * 16);
      const u32x4 kfrag = *(const u32x4*)(smem + L_F + 512 + r * 64 + q * 16);
      const char* kt = smem + L_KT + (s & 1) * 2048 + i * 64 + q * 16;
      const bf16x8 kt0 = *(const bf16x8*)kt, kt1 = *(const bf16x8*)(kt + 1024);
      const f32x4 z = {0, 0, 0, 0};
      const f32x4 c1 = mfma16(kt0, asfrag(qf), z);
      const f32x4 c2 = mfma16(kt1, asfrag(qf), z);
      const f32x4 c0 = mfma16(asfrag(kfrag), asfrag(qf), z);
      if (s < NSEG - 1) {
        const u32x4 v12 = {nos(__float_as_uint(g2 ? c2[0] : c1[0])), nos(__float_as_uint(g2 ? c2[1] : c1[1])), nos(__float_as_uint(g2 ? c2[2] : c1[2])), nos(__float_as_uint(g2 ? c2[3] : c1[3]))};
        stx(rs, oS, sb + jo, v12);
        const u32x4 v0 = {nos(__float_as_uint(c0[0])), nos(__float_as_uint(c0[1])), nos(__float_as_uint(c0[2])), nos(__float_as_uint(c0[3]))};
        if (l < 32 && g2 == 0) stx(rs, pS0, sb, v0);
      }
      if (g2 == 0) __builtin_amdgcn_raw_buffer_store_b128(kfrag, rkm, (r * HD + 8 * q) * 2, s * 8 * HD * 2, 16);
    }
    TS(3);
#pragma unroll
    for (int ks = 0; ks < 4; ++ks) { acc[4] = mfma16(W1[4][ks], asfrag(mv[ks]), ks ? acc[4] : z4); acc[5] = mfma16(W1[5][ks], asfrag(mv[ks]), ks ? acc[5] : z4); }
    {
      f32x4 a;
      a = halfsum4(acc[4], acc[5]);
      *(f32x4*)(RV + w * 296 + r * 36 + 16 * g2 + 4 * q) = a;
    }
    u32x4 sv[5];
    if (w == SMW && s < NSEG - 1) {
      nap(d2);
#pragma unroll
      for (int k = 0; k < 5; ++k) sv[k] = k < 4 ? ldx(rs, oS + k * 1024, sb) : ldx(rs, oS0, sb);
    }
    BARL();
    if (PFPOS == 2 && w == 7 && s + 1 < NSEG) prefetch(s + 1);
    if (RSPOS == 2) RESET();
    TS(4);
    if (w == SMW) {
      f32x4 rd[4][2];
#pragma unroll
      for (int ww = 0; ww < 4; ++ww) { const float* b = RV + (4 * g2 + ww) * 296 + r * 36 + 8 * q; rd[ww][0] = *(const f32x4*)b; rd[ww][1] = *(const f32x4*)(b + 4); }
      __builtin_amdgcn_sched_barrier(0);
      f32x4 s0 = (rd[0][0] + rd[1][0]) + (rd[2][0] + rd[3][0]), s1 = (rd[0][1] + rd[1][1]) + (rd[2][1] + rd[3][1]);
#pragma unroll
      for (int e = 0; e < 4; ++e) { s0[e] += ror8(s0[e]); s1[e] += ror8(s1[e]); }
      const u32x4 vf = {cvt_pk(s0[0], s0[1]), cvt_pk(s0[2], s0[3]), cvt_pk(s1[0], s1[1]), cvt_pk(s1[2], s1[3])};
      if (g2 == 0) *(u32x4*)(smem + L_VM + r * 64 + q * 16) = vf;
      if (s < NSEG - 1) {
        const char* vt = smem + L_VT + (s & 1) * 2048 + (4 * q + (i >> 2)) * 64 + (i & 3) * 16;
        const char* vm = smem + L_VM + (4 * q + (i >> 2)) * 64 + (i & 3) * 16;
        const bf16x8 vt0 = cat4(trread(vt), trread(vt + 1024)), vt1 = cat4(trread(vt + 8), trread(vt + 1024 + 8));
        const s16x4 zz = {0, 0, 0, 0};
        const bf16x8 vm0 = cat4(trread(vm), zz), vm1 = cat4(trread(vm + 8), zz);
        TS(5);
        {
          u32 mxv = 0;
#pragma unroll
          for (int k = 0; k < 5; ++k) mxv = max(max(mxv, max(sv[k].x, sv[k].y)), max(sv[k].z, sv[k].w));
          if (!__all(mxv != SENT)) { ++ns2; d2 = min(d2 + CH_INC, CH_D2 + 10); POLL(5, sv, (k < 4 ? ldx(rs, oS + k * 1024, sb) : ldx(rs, oS0, sb)), ns2); }
        }
        TS(6);
        const float sc = 0.08838834764831845f * 1.4426950408889634f;
        float st[4], sm[4];
        st[0] = (__uint_as_float(sv[0].x) + __uint_as_float(sv[1].x)) + (__uint_as_float(sv[2].x) + __uint_as_float(sv[3].x));
        st[1] = (__uint_as_float(sv[0].y) + __uint_as_float(sv[1].y)) + (__uint_as_float(sv[2].y) + __uint_as_float(sv[3].y));
        st[2] = (__uint_as_float(sv[0].z) + __uint_as_float(sv[1].z)) + (__uint_as_float(sv[2].z) + __uint_as_float(sv[3].z));
        st[3] = (__uint_as_float(sv[0].w) + __uint_as_float(sv[1].w)) + (__uint_as_float(sv[2].w) + __uint_as_float(sv[3].w));
        sm[0] = __uint_as_float(sv[4].x); sm[1] = __uint_as_float(sv[4].y); sm[2] = __uint_as_float(sv[4].z); sm[3] = __uint_as_float(sv[4].w);
#pragma unroll
        for (int e = 0; e < 4; ++e) sm[e] = red32_add(sm[e] + ror8(sm[e]));
        float mx = fmaxf(fmaxf(fmaxf(st[0], st[1]), fmaxf(st[2], st[3])), fmaxf(fmaxf(sm[0], sm[1]), fmaxf(sm[2], sm[3])));
        mx = fmaxf(mx, ror8(mx)); mx = red32_max(red16_max(mx));
        const float nm = -mx * sc;
#pragma unroll
        for (int e = 0; e < 4; ++e) { st[e] = __builtin_amdgcn_exp2f(fmaf(st[e], sc, nm)); sm[e] = __builtin_amdgcn_exp2f(fmaf(sm[e], sc, nm)); }
        const u32 p0 = cvt_pk(st[0], st[1]), p1 = cvt_pk(st[2], st[3]);
        const u32 m0 = cvt_pk(sm[0], sm[1]), m1 = cvt_pk(sm[2], sm[3]);
        const u32x4 pt = {p0, p1, ror8u(p0), ror8u(p1)};
        const u32x4 pm = {q < 2 ? m0 : 0u, q < 2 ? m1 : 0u, 0u, 0u};
        const f32x4 z = {0, 0, 0, 0};
        const f32x4 o0 = mfma16(vm0, asfrag(pm), mfma16(vt0, asfrag(pt), z)), o1 = mfma16(vm1, asfrag(pm), mfma16(vt1, asfrag(pt), z));
        float ts = (bf_lo(p0) + bf_hi(p0)) + (bf_lo(p1) + bf_hi(p1)), ms = (bf_lo(m0) + bf_hi(m0)) + (bf_lo(m1) + bf_hi(m1));
        ts += ror8(ts); ts = red32_add(red16_add(ts)); ms = red16_add(ms);
        const float inv = __builtin_amdgcn_rcpf(ts + ms);
        const u32x4 ov = {nos(cvt_pk(o0[0] * inv, o0[1] * inv)), nos(cvt_pk(o0[2] * inv, o0[3] * inv)), nos(cvt_pk(o1[0] * inv, o1[1] * inv)), nos(cvt_pk(o1[2] * inv, o1[3] * inv))};
        if (g2 == 0) stx(rs, pO, sb, ov);
      }
      if (g2 == 0) __builtin_amdgcn_raw_buffer_store_b128(vf, rkm, (4u << 20) + (r * HD + 8 * q) * 2, s * 8 * HD * 2, 16);
    }
    TS(7);
    if (s == NSEG - 1) {
      asm volatile("s_waitcnt vmcnt(0)" ::: "memory");
      BARL();
      if (tid == 0) ctl_add(ctl + C_KM + s * 32 + h, 1);
      break;
    }
    BARL();
    if (PFPOS == 1 && w == 7) prefetch(s + 1);
    if (RSPOS == 1) RESET();
    u32x4 ov[4];
    bf16x8 W2[8];
#pragma unroll
    for (int ks = 0; ks < 8; ++ks) W2[ks] = *(const bf16x8*)(smem + L_W2 + (w * 8 + ks) * 1024 + l * 16);
    nap(d3); TS(8);
    f32x4 a0, a1;
    {
#pragma unroll
      for (int k = 0; k < 4; ++k) ov[k] = ldx(rs, oO + k * 1024, sb);
#pragma unroll
      for (int k = 0; k < 4; ++k) { a0 = mfma16(W2[2 * k], asfrag(ov[k]), k ? a0 : z4); a1 = mfma16(W2[2 * k + 1], asfrag(ov[k]), k ? a1 : z4); }
      u32 mxv = 0;
#pragma unroll
      for (int k = 0; k < 4; ++k) mxv = max(max(mxv, max(ov[k].x, ov[k].y)), max(ov[k].z, ov[k].w));
      if (!__all(mxv != SENT)) {
        ++ns3; d3 = min(d3 + CH_INC, CH_D3 + 10);
        POLL(4, ov, ldx(rs, oO + k * 1024, sb), ns3);
#pragma unroll
        for (int k = 0; k < 4; ++k) { a0 = mfma16(W2[2 * k], asfrag(ov[k]), k ? a0 : z4); a1 = mfma16(W2[2 * k + 1], asfrag(ov[k]), k ? a1 : z4); }
      }
    }
    TS(9);
    {
      f32x4 a;
#pragma unroll
      for (int e = 0; e < 4; ++e) a[e] = a0[e] + ror8(a1[e]);
      if (g2 == 0) *(f32x4*)(R2 + w * 160 + r * 20 + 4 * q) = a;
    }
    asm volatile("s_waitcnt vmcnt(0)" ::: "memory");
    BARL();
    if (tid == 192) ctl_add(ctl + C_KM + s * 32 + h, 1);
    TS(10);
    if (w == 1) {
      const int n2 = l & 7, rr = l >> 3;
      f32x2 rr8[8];
#pragma unroll
      for (int ww = 0; ww < 8; ++ww) rr8[ww] = *(const f32x2*)(R2 + ww * 160 + rr * 20 + 2 * n2);
      __builtin_amdgcn_sched_barrier(0);
      const f32x2 sum = ((rr8[0] + rr8[1]) + (rr8[2] + rr8[3])) + ((rr8[4] + rr8[5]) + (rr8[6] + rr8[7]));
      const u32 v = nos(cvt_pk(sum[0], sum[1]));
      const u32x4 o = {(u32)__builtin_amdgcn_update_dpp(0, (int)v, 0x00, 0xF, 0xF, true), (u32)__builtin_amdgcn_update_dpp(0, (int)v, 0x55, 0xF, 0xF, true),
                       (u32)__builtin_amdgcn_update_dpp(0, (int)v, 0xAA, 0xF, 0xF, true), (u32)__builtin_amdgcn_update_dpp(0, (int)v, 0xFF, 0xF, 0xF, true)};
      if ((l & 3) == 0) stx(rs, pM, (u32)((s + 1) & 3) * (2 * SLOT), o);
    }
  }
}
constexpr int G_HT = 128 * 64;
DEV int lds_byte(int r, int c) { int st = (r >> 4) * 2 + (c >> 5), rr = r & 15, cc = c & 31, ob = rr * 64 + cc * 2; return st * 1024 + (ob ^ (((ob >> 9) & 1) << 5)); }
DEV void stage_rc(int b, int& R, int& C) { int st = b / 1024, sb = b % 1024, swz = sb ^ (((sb >> 9) & 1) << 5); R = (st >> 1) * 16 + swz / 64; C = (st & 1) * 32 + (swz % 64) / 2; }
DEV int swzb(int row) { return ((row & 3) << 2) | ((row >> 2) & 3); }
DEV float mish(float h) {
  const float u = __expf(-fabsf(h));
  const float n = h >= 0.f ? 1.f + 2.f * u : u * (u + 2.f);
  const float d = h >= 0.f ? n + 2.f * u * u : n + 2.f;
  return h * n * __builtin_amdgcn_rcpf(d);
}
DEV void gemm256(const int wv, const int MODE, const u16* __restrict__ A, const int lda, const u16* __restrict__ B, const int ldb, const int K, void* C, const int ldc, const u16* bias, char* smem) {
  u16* shm = (u16*)smem;
#define SA(b, h) (shm + ((b) * 2 + (h)) * G_HT)
#define SB(b, h) (shm + (4 + (b) * 2 + (h)) * G_HT)
#define STA_X(T, VO, P, br, kt) do { const int _s = ((br) * lda + (kt) * 64) * 2; \
    __builtin_amdgcn_raw_ptr_buffer_load_lds(rsa, (LDSP void*)((char*)(P) + (T) * 16), 16, (VO), _s, 0, 0); \
    __builtin_amdgcn_raw_ptr_buffer_load_lds(rsa, (LDSP void*)((char*)(P) + (T) * 16 + 8192), 16, (VO), _s + 128 * lda, 0, 0); } while (0)
#define STB_X(T, VO, P, bc, kt) do { const int _s = ((kt) * 64 * ldb + (bc)) * 2; \
    __builtin_amdgcn_raw_ptr_buffer_load_lds(rsb, (LDSP void*)((char*)(P) + (T) * 16), 16, (VO), _s, 0, 0); \
    __builtin_amdgcn_raw_ptr_buffer_load_lds(rsb, (LDSP void*)((char*)(P) + (T) * 16 + 8192), 16, (VO), _s + 64 * ldb, 0, 0); } while (0)
#define STA(P, br, kt) STA_X(t0, voa, P, br, kt)
#define STB(P, bc, kt) STB_X(t0, vob, P, bc, kt)
#define LDA(dst, b, h) for (int m = 0; m < 4; ++m) for (int k = 0; k < 2; ++k) dst[m][k] = *reinterpret_cast<const bf16x8*>((char*)SA(b, h) + lds_byte(wr * 64 + m * 16 + fr, k * 32 + fq * 8))
#define LDB(dst, b, h) for (int n = 0; n < 2; ++n) for (int k = 0; k < 2; ++k) { const char* _p = (const char*)SB(b, h) + k * 8192; \
      dst[n][k] = cat4(trread(_p + bo[n][0]), trread(_p + bo[n][1])); }
#define MMA(ai, bj, At, Bt) do { __builtin_amdgcn_s_setprio(1); \
    for (int m = 0; m < 4; ++m) for (int n = 0; n < 2; ++n) for (int k = 0; k < 2; ++k) acc[ai][bj][m][n] = mfma16(Bt[n][k], At[m][k], acc[ai][bj][m][n]); \
    __builtin_amdgcn_s_setprio(0); } while (0)
#define WAIT_V(n) asm volatile("s_waitcnt vmcnt(" #n ")" ::: "memory")
#define WAIT_L(n) asm volatile("s_waitcnt lgkmcnt(" #n ")" ::: "memory")
#define BAR __builtin_amdgcn_s_barrier()
#define SCHED __builtin_amdgcn_sched_barrier(0)
  const int t0 = tid_of(wv);
  int wid = t0 >> 6, lane = t0 & 63, wr = wid >> 2, wc = wid & 3, fr = lane & 15, fq = lane >> 4;
  f32x4 acc[2][2][4][2] = {};
  bf16x8 At[4][2], B0[2][2], B1[2][2];
  int bo[2][2];
  for (int n = 0; n < 2; ++n) for (int t = 0; t < 2; ++t) { const int row = 8 * fq + 4 * t + (fr >> 2), ch = 4 * wc + (fr & 3); bo[n][t] = 256 * row + 16 * (ch ^ swzb(row)) + 8 * n; }
  const int nt = K / 64;
  const __amdgpu_buffer_rsrc_t rsa = __builtin_amdgcn_make_buffer_rsrc((void*)uni(A), 0, 0x7FFFFFFF, 0x00020000), rsb = __builtin_amdgcn_make_buffer_rsrc((void*)uni(B), 0, 0x7FFFFFFF, 0x00020000);
  u32 voa, vob;
  { int _r, _c; stage_rc(t0 * 16, _r, _c); voa = (u32)(_r * lda + _c) * 2u; const int _b = t0 * 16, _rb = _b >> 8; vob = (u32)(_rb * ldb + ((((_b >> 4) & 15) ^ swzb(_rb)) * 8)) * 2u; }
  STB(SB(0, 0), 0, 0); STA(SA(0, 0), 0, 0); STB(SB(0, 1), 128, 0); STA(SA(0, 1), 128, 0);
  if (wr == 1) BAR;
  WAIT_V(4); BAR;
  STB(SB(1, 0), 0, 1); STA(SA(1, 0), 0, 1); STB(SB(1, 1), 128, 1);
  WAIT_V(6); BAR;
  for (int t = 0; t < nt - 2; t += 2) {
    LDB(B0, 0, 0); SCHED; LDA(At, 0, 0); STA(SA(1, 1), 128, t + 1);
    WAIT_L(8); BAR; WAIT_L(0); MMA(0, 0, At, B0); BAR; SCHED;
    LDB(B1, 0, 1); STB(SB(0, 0), 0, t + 2);
    BAR; WAIT_L(0); MMA(0, 1, At, B1); BAR;
    LDA(At, 0, 1); STA(SA(0, 0), 0, t + 2);
    BAR; WAIT_L(0); MMA(1, 0, At, B0); BAR; SCHED;
    STB(SB(0, 1), 128, t + 2);
    WAIT_V(6); BAR; MMA(1, 1, At, B1); BAR;
    LDB(B0, 1, 0); SCHED; LDA(At, 1, 0); STA(SA(0, 1), 128, t + 2);
    WAIT_L(8); BAR; WAIT_L(0); MMA(0, 0, At, B0); BAR; SCHED;
    LDB(B1, 1, 1); STB(SB(1, 0), 0, t + 3);
    BAR; WAIT_L(0); MMA(0, 1, At, B1); BAR;
    LDA(At, 1, 1); STA(SA(1, 0), 0, t + 3);
    BAR; WAIT_L(0); MMA(1, 0, At, B0); BAR; SCHED;
    STB(SB(1, 1), 128, t + 3);
    WAIT_V(6); BAR; MMA(1, 1, At, B1); BAR;
  }
  int tz = tid_of(wv); wid = tz >> 6; lane = tz & 63; wr = wid >> 2; wc = wid & 3; fr = lane & 15; fq = lane >> 4;
  { LDB(B0, 0, 0); WAIT_V(0); LDA(At, 0, 0); { int _r, _c; stage_rc(tz * 16, _r, _c); const u32 voa2 = (u32)(_r * lda + _c) * 2u; STA_X(tz, voa2, SA(1, 1), 128, nt - 1); }
    BAR; WAIT_L(0); MMA(0, 0, At, B0); BAR;
    LDB(B1, 0, 1); BAR; WAIT_L(0); MMA(0, 1, At, B1); BAR;
    LDA(At, 0, 1); WAIT_V(4); BAR; WAIT_L(0); MMA(1, 0, At, B0); MMA(1, 1, At, B1); BAR; }
  { LDB(B0, 1, 0); LDA(At, 1, 0); WAIT_V(2); BAR; WAIT_L(0); MMA(0, 0, At, B0); BAR;
    LDB(B1, 1, 1); WAIT_V(0); BAR; WAIT_L(0); MMA(0, 1, At, B1); BAR;
    LDA(At, 1, 1); BAR; WAIT_L(0); MMA(1, 0, At, B0); MMA(1, 1, At, B1); BAR; }
  if (wr == 0) BAR;
  const __amdgpu_buffer_rsrc_t rc = __builtin_amdgcn_make_buffer_rsrc((void*)uni((char*)C), 0, 0x7FFFFFFF, 0x00020000);
#pragma unroll
  for (int ai = 0; ai < 2; ++ai)
#pragma unroll
  for (int bj = 0; bj < 2; ++bj)
#pragma unroll
  for (int m = 0; m < 4; ++m) {
    const int row = ai * 128 + wr * 64 + m * 16 + fr, col = bj * 128 + wc * 32 + fq * 8;
    f32x4 v0 = acc[ai][bj][m][0], v1 = acc[ai][bj][m][1];
    if (MODE == 4) {
      const __amdgpu_buffer_rsrc_t ry = __builtin_amdgcn_make_buffer_rsrc((void*)bias, 0, 0x7FFFFFFF, 0x00020000);
      const u32 o = (u32)(row * ldc + col) * 4;
      v0 += __builtin_bit_cast(f32x4, __builtin_amdgcn_raw_buffer_load_b128(ry, o, 0, 16)) + __builtin_bit_cast(f32x4, __builtin_amdgcn_raw_buffer_load_b128(ry, o, 8u << 20, 16));
      v1 += __builtin_bit_cast(f32x4, __builtin_amdgcn_raw_buffer_load_b128(ry, o + 16, 0, 16)) + __builtin_bit_cast(f32x4, __builtin_amdgcn_raw_buffer_load_b128(ry, o + 16, 8u << 20, 16));
      __builtin_amdgcn_raw_buffer_store_b128(u32x4{cvt_pk(v0[0], v0[1]), cvt_pk(v0[2], v0[3]), cvt_pk(v1[0], v1[1]), cvt_pk(v1[2], v1[3])}, rc, (row * ldc + col) * 2, 0, 0);
    } else if (MODE >= 2) {
      if (MODE == 2) { const u32x4 xv = *(const u32x4*)(bias + (long)row * ldc + col);
        v0[0] += bf_lo(xv.x); v0[1] += bf_hi(xv.x); v0[2] += bf_lo(xv.y); v0[3] += bf_hi(xv.y); v1[0] += bf_lo(xv.z); v1[1] += bf_hi(xv.z); v1[2] += bf_lo(xv.w); v1[3] += bf_hi(xv.w); }
      __builtin_amdgcn_raw_buffer_store_b128(__builtin_bit_cast(u32x4, v0), rc, (row * ldc + col) * 4, 0, 16);
      __builtin_amdgcn_raw_buffer_store_b128(__builtin_bit_cast(u32x4, v1), rc, (row * ldc + col) * 4 + 16, 0, 16);
    } else {
      if (MODE == 1) { const u32x4 bb = *(const u32x4*)(bias + col);
        v0[0] = mish(v0[0] + bf_lo(bb.x)); v0[1] = mish(v0[1] + bf_hi(bb.x)); v0[2] = mish(v0[2] + bf_lo(bb.y)); v0[3] = mish(v0[3] + bf_hi(bb.y));
        v1[0] = mish(v1[0] + bf_lo(bb.z)); v1[1] = mish(v1[1] + bf_hi(bb.z)); v1[2] = mish(v1[2] + bf_lo(bb.w)); v1[3] = mish(v1[3] + bf_hi(bb.w)); }
      __builtin_amdgcn_raw_buffer_store_b128(u32x4{cvt_pk(v0[0], v0[1]), cvt_pk(v0[2], v0[3]), cvt_pk(v1[0], v1[1]), cvt_pk(v1[2], v1[3])}, rc, (row * ldc + col) * 2, 0, 16);
    }
  }
#undef SA
#undef SB
#undef LDA
#undef LDB
#undef MMA
}
DEV void wait_cnts(const int wv, u32* ctl, int ia, int na, u32 ta, int ib, int nb, u32 tb);
DEV void attn_item(const int wv, const Params& p, int s, int h, char* lds) {
  const int t0 = tid_of(0);
  const int l = t0 & 63, q = l >> 4, i = l & 15;
  const __amdgpu_buffer_rsrc_t rq = __builtin_amdgcn_make_buffer_rsrc((void*)((const u16*)(p.ws + WS_Q) + (size_t)s * 32 * HD + h * 128), 0, 0x7FFFFFFF, 0x00020000);
  const __amdgpu_buffer_rsrc_t rm = __builtin_amdgcn_make_buffer_rsrc((void*)((const u16*)(p.ws + WS_KM) + (size_t)s * 8 * HD + h * 128), 0, 0x7FFFFFFF, 0x00020000);
  const __amdgpu_buffer_rsrc_t rk = __builtin_amdgcn_make_buffer_rsrc((void*)((const u16*)(p.ws + WS_K) + (size_t)s * 32 * HD + h * 128), 0, 0x7FFFFFFF, 0x00020000);
  u32x4 vv[8]; bf16x8 qf[4][2], kf[4][2];
#pragma unroll
  for (int it = 0; it < 8; ++it) vv[it] = __builtin_amdgcn_raw_buffer_load_b128(rk, ((it * 4 + q) * HD + i * 8) * 2, (u32)(WS_V - WS_K), 16);
#pragma unroll
  for (int ks = 0; ks < 4; ++ks) {
    const int e = ks * 32 + q * 8;
    qf[ks][0] = asfrag(__builtin_amdgcn_raw_buffer_load_b128(rq, (i * HD + e) * 2, 0, 16)); qf[ks][1] = asfrag(__builtin_amdgcn_raw_buffer_load_b128(rq, ((16 + i) * HD + e) * 2, 0, 16));
    kf[ks][0] = asfrag(__builtin_amdgcn_raw_buffer_load_b128(rk, (i * HD + e) * 2, 0, 16)); kf[ks][1] = asfrag(__builtin_amdgcn_raw_buffer_load_b128(rk, ((16 + i) * HD + e) * 2, 0, 16));
  }
  __builtin_amdgcn_sched_barrier(0);
#pragma unroll
  for (int it = 0; it < 8; ++it) { const int row = 16 + it * 4 + q; *(u32x4*)(lds + row * 256 + 16 * (i ^ swzb(row))) = vv[it]; }
#pragma unroll
  for (int it = 2; it < 4; ++it) { const int row = it * 4 + q; *(u32x4*)(lds + row * 256 + 16 * (i ^ swzb(row))) = u32x4{0, 0, 0, 0}; }
  f32x4 c[3][2];
#pragma unroll
  for (int t = 0; t < 3; ++t) c[t][0] = c[t][1] = f32x4{0, 0, 0, 0};
#pragma unroll
  for (int ks = 0; ks < 4; ++ks) { c[1][0] = mfma16(kf[ks][0], qf[ks][0], c[1][0]); c[1][1] = mfma16(kf[ks][0], qf[ks][1], c[1][1]); c[2][1] = mfma16(kf[ks][1], qf[ks][1], c[2][1]); }
  wait_cnts(wv, (u32*)(p.ws + WS_CTL), C_KM + s * 32 + (h - wv), 8, 4, 0, 0, 0);
  __syncthreads();
  {
    u32x4 vm[2]; bf16x8 km[4];
#pragma unroll
    for (int it = 0; it < 2; ++it) vm[it] = __builtin_amdgcn_raw_buffer_load_b128(rm, (4u << 20) + ((it * 4 + q) * HD + i * 8) * 2, 0, 16);
#pragma unroll
    for (int ks = 0; ks < 4; ++ks) km[ks] = asfrag(__builtin_amdgcn_raw_buffer_load_b128(rm, ((i & 7) * HD + ks * 32 + q * 8) * 2, 0, 16));
#pragma unroll
    for (int it = 0; it < 2; ++it) { const int row = it * 4 + q; *(u32x4*)(lds + row * 256 + 16 * (i ^ swzb(row))) = vm[it]; }
#pragma unroll
    for (int ks = 0; ks < 4; ++ks) { c[0][0] = mfma16(km[ks], qf[ks][0], c[0][0]); c[0][1] = mfma16(km[ks], qf[ks][1], c[0][1]); }
  }
  const float sc = 0.08838834764831845f * 1.4426950408889634f, NEG = -3.0e38f;
  u32x4 pt[2], pm[2];
#pragma unroll
  for (int u = 0; u < 2; ++u) {
    float sm[4], s1[4], s2[4]; float mx = NEG;
#pragma unroll
    for (int e = 0; e < 4; ++e) {
      sm[e] = q < 2 ? c[0][u][e] * sc : NEG;
      s1[e] = (u == 1 || 4 * q + e <= i) ? c[1][u][e] * sc : NEG;
      s2[e] = (u == 1 && 4 * q + e <= i) ? c[2][u][e] * sc : NEG;
      mx = fmaxf(mx, fmaxf(sm[e], fmaxf(s1[e], s2[e])));
    }
    mx = red32_max(red16_max(mx));
    float sum = 0;
#pragma unroll
    for (int e = 0; e < 4; ++e) {
      sm[e] = q < 2 ? __builtin_amdgcn_exp2f(sm[e] - mx) : 0.f;
      s1[e] = (u == 1 || 4 * q + e <= i) ? __builtin_amdgcn_exp2f(s1[e] - mx) : 0.f;
      s2[e] = (u == 1 && 4 * q + e <= i) ? __builtin_amdgcn_exp2f(s2[e] - mx) : 0.f;
      sum += sm[e] + s1[e] + s2[e];
    }
    sum = red32_add(red16_add(sum));
    const float inv = 1.f / sum;
    pt[u] = u32x4{cvt_pk(s1[0] * inv, s1[1] * inv), cvt_pk(s1[2] * inv, s1[3] * inv), cvt_pk(s2[0] * inv, s2[1] * inv), cvt_pk(s2[2] * inv, s2[3] * inv)};
    pm[u] = u32x4{cvt_pk(sm[0] * inv, sm[1] * inv), cvt_pk(sm[2] * inv, sm[3] * inv), 0u, 0u};
  }
  asm volatile("s_waitcnt lgkmcnt(0)" ::: "memory");
  u16* O = (u16*)(p.ws + WS_O) + (size_t)s * 32 * HD + h * 128;
  const __amdgpu_buffer_rsrc_t ro = __builtin_amdgcn_make_buffer_rsrc((void*)O, 0, 0x7FFFFFFF, 0x00020000);
  const s16x4 zz = {0, 0, 0, 0};
#pragma unroll
  for (int cp = 0; cp < 4; ++cp) {
    f32x4 o[2][2];
#pragma unroll
    for (int c2 = 0; c2 < 2; ++c2) {
      const int ch = 4 * cp + (i & 3);
      const int rm = 4 * q + (i >> 2), r1 = 16 + rm, r2 = 32 + rm;
      const bf16x8 vm = cat4(trread(lds + rm * 256 + 16 * (ch ^ swzb(rm)) + 8 * c2), zz);
      const bf16x8 vt = cat4(trread(lds + r1 * 256 + 16 * (ch ^ swzb(r1)) + 8 * c2), trread(lds + r2 * 256 + 16 * (ch ^ swzb(r2)) + 8 * c2));
#pragma unroll
      for (int u = 0; u < 2; ++u) o[c2][u] = mfma16(vm, asfrag(pm[u]), mfma16(vt, asfrag(pt[u]), f32x4{0, 0, 0, 0}));
    }
#pragma unroll
    for (int u = 0; u < 2; ++u)
      __builtin_amdgcn_raw_buffer_store_b128(u32x4{cvt_pk(o[0][u][0], o[0][u][1]), cvt_pk(o[0][u][2], o[0][u][3]), cvt_pk(o[1][u][0], o[1][u][1]), cvt_pk(o[1][u][2], o[1][u][3])}, ro, ((16 * u + i) * HD + 32 * cp + 8 * q) * 2, 0, 16);
  }
}

template <int BM, int LDA, int LDB, int AUXA>
DEV void small_gemm(const int wv, const u16* A, const u16* B, f32x4 (&acc)[BM / 32][2], char* smem) {
  constexpr int MT = BM / 32, NLD = BM / 64 + 2, NS = (BM == 128) ? 4 : 5;
  const int tid = tid_of(wv);
  const int lane = tid & 63, wr = wv >> 2, wc = wv & 3, fr = lane & 15, fq = lane >> 4;
  const __amdgpu_buffer_rsrc_t rsa = __builtin_amdgcn_make_buffer_rsrc((void*)uni(A), 0, 0x7FFFFFFF, 0x00020000), rsb = __builtin_amdgcn_make_buffer_rsrc((void*)uni(B), 0, 0x7FFFFFFF, 0x00020000);
  u32 voa, vob;
  { int r, c; stage_rc(tid * 16, r, c); voa = (u32)(r * LDA + c) * 2u; const int b = tid * 16, rb = b >> 8; vob = (u32)(rb * LDB + ((((b >> 4) & 15) ^ swzb(rb)) * 8)) * 2u; }
  constexpr int STG = BM * 128 + 16384;
  auto stage = [&](int kt, int slot) {
    char* sa = smem + slot * STG + tid * 16;
    __builtin_amdgcn_raw_ptr_buffer_load_lds(rsa, (LDSP void*)sa, 16, voa, kt * 128, 0, AUXA);
    if (BM == 128) __builtin_amdgcn_raw_ptr_buffer_load_lds(rsa, (LDSP void*)(sa + 8192), 16, voa, kt * 128 + 64 * LDA * 2, 0, AUXA);
    __builtin_amdgcn_raw_ptr_buffer_load_lds(rsb, (LDSP void*)(sa + BM * 128), 16, vob, kt * 64 * LDB * 2, 0, 0);
    __builtin_amdgcn_raw_ptr_buffer_load_lds(rsb, (LDSP void*)(sa + BM * 128 + 8192), 16, vob, (kt * 64 + 32) * LDB * 2, 0, 0);
  };
  int bo[2];
#pragma unroll
  for (int t = 0; t < 2; ++t) { const int row = 8 * fq + 4 * t + (fr >> 2); bo[t] = BM * 128 + 256 * row + 16 * ((4 * wc + (fr & 3)) ^ swzb(row)); }
  const int ao = lds_byte(wr * (BM / 2) + fr, fq * 8);
#pragma unroll
  for (int m = 0; m < MT; ++m) acc[m][0] = acc[m][1] = f32x4{0, 0, 0, 0};
#pragma unroll
  for (int k = 0; k < NS - 1; ++k) stage(k, k);
  constexpr int nt = 16;
  int cur = 0, nxt = NS - 1;
  for (int kt = 0; kt < nt; ++kt) {
    if (kt + NS - 2 < nt) asm volatile("s_waitcnt vmcnt(%0)" ::"n"((NS - 2) * NLD) : "memory"); else asm volatile("s_waitcnt vmcnt(0)" ::: "memory");
    __builtin_amdgcn_s_barrier();
    if (kt + NS - 1 < nt) stage(kt + NS - 1, nxt);
    const char* sa = smem + cur * STG;
#pragma unroll
    for (int k = 0; k < 2; ++k) {
      const bf16x8 b0 = cat4(trread(sa + k * 8192 + bo[0]), trread(sa + k * 8192 + bo[1])), b1 = cat4(trread(sa + k * 8192 + bo[0] + 8), trread(sa + k * 8192 + bo[1] + 8));
#pragma unroll
      for (int m = 0; m < MT; ++m) {
        const bf16x8 a = *(const bf16x8*)(sa + ao + m * 2048 + k * 1024);
        acc[m][0] = mfma16(b0, a, acc[m][0]); acc[m][1] = mfma16(b1, a, acc[m][1]);
      }
    }
    asm volatile("s_waitcnt lgkmcnt(0)" ::: "memory");
    cur = cur == NS - 1 ? 0 : cur + 1; nxt = nxt == NS - 1 ? 0 : nxt + 1;
  }
}
template <int BM>
DEV void wo_item(const int wv, const Params& p, int g, int nt_, int kq, char* smem) {
  constexpr int MT = BM / 32;
  f32x4 acc[MT][2];
  small_gemm<BM, HD, D_, 16>(wv, (const u16*)(p.ws + WS_O) + (size_t)g * BM * HD + kq * 1024, p.Wo + (size_t)kq * 1024 * D_ + nt_ * 128, acc, smem);
  const int tid = tid_of(wv);
  const int lane = tid & 63, wr = wv >> 2, wc = wv & 3, fr = lane & 15, fq = lane >> 4;
  const int tile = (BM == 128 ? g : g - 15) * 8 + nt_;
  char* part = p.ws + WS_P + (size_t)tile * (4 * 65536);
  const __amdgpu_buffer_rsrc_t rp = __builtin_amdgcn_make_buffer_rsrc((void*)part, 0, 0x7FFFFFFF, 0x00020000);
#pragma unroll
  for (int m = 0; m < MT; ++m)
#pragma unroll
    for (int n = 0; n < 2; ++n) __builtin_amdgcn_raw_buffer_store_b128(__builtin_bit_cast(u32x4, acc[m][n]), rp, tid * 16 + (m * 2 + n) * 8192, kq * 65536, 16);
  asm volatile("s_waitcnt vmcnt(0)" ::: "memory");
  __syncthreads();
  volatile int* s_last = (volatile int*)(smem + 128 * 1024 + 32);
  if (tid == 0) *s_last = (int)ctl_add((u32*)(p.ws + WS_CTL) + C_P + tile, 1);
  __syncthreads();
  if (__builtin_amdgcn_readfirstlane(*s_last) != 3) return;
  if (wv == 0) {
    u32* cy = (u32*)(p.ws + WS_CTL) + C_Y + (g * BM >> 8) * 4 + (nt_ >> 1);
    for (int sp = 0; __builtin_amdgcn_readfirstlane(ctl_ld(cy)) < 2; ++sp) { __builtin_amdgcn_s_sleep(6); if (sp > 400000) { ctl_st((u32*)(p.ws + WS_CTL) + C_TMO, 1); break; } }
  }
  __syncthreads();
  const float* Y1 = (const float*)(p.ws + WS_Y); const float* Y2 = (const float*)(p.ws + WS_Y + (8u << 20));
  const __amdgpu_buffer_rsrc_t ry = __builtin_amdgcn_make_buffer_rsrc((void*)Y1, 0, 0x7FFFFFFF, 0x00020000);
#pragma unroll
  for (int m = 0; m < MT; ++m) {
    const u32 o = (u32)(g * BM + wr * (BM / 2) + m * 16 + fr) * D_ + nt_ * 128 + wc * 32 + fq * 8;
    f32x4 v[2];
#pragma unroll
    for (int n = 0; n < 2; ++n) {
      f32x4 t[4];
#pragma unroll
      for (int k = 0; k < 4; ++k) t[k] = __builtin_bit_cast(f32x4, __builtin_amdgcn_raw_buffer_load_b128(rp, tid * 16 + (m * 2 + n) * 8192, k * 65536, 16));
      const f32x4 y1 = __builtin_bit_cast(f32x4, __builtin_amdgcn_raw_buffer_load_b128(ry, o * 4 + n * 16, 0, 16)), y2 = __builtin_bit_cast(f32x4, __builtin_amdgcn_raw_buffer_load_b128(ry, o * 4 + n * 16, 8u << 20, 16));
      v[n] = ((t[0] + t[1]) + (t[2] + t[3])) + (y1 + y2);
    }
    *(u32x4*)(p.out + o) = u32x4{cvt_pk(v[0][0], v[0][1]), cvt_pk(v[0][2], v[0][3]), cvt_pk(v[1][0], v[1][1]), cvt_pk(v[1][2], v[1][3])};
  }
}

DEV void kv0_item(const int wv, const Params& p, int j, char* smem) {
  const int hf = j & 1, mat = (j >> 1) & 1, n0 = (j >> 2) * 128;
  f32x4 acc[4][2];
  small_gemm<128, D_, HD, 0>(wv, (const u16*)(p.ws + WS_XN) + (size_t)hf * 128 * D_, (mat ? p.Wv : p.Wk) + n0, acc, smem);
  const int tid = tid_of(wv), lane = tid & 63, wr = wv >> 2, wc = wv & 3, fr = lane & 15, fq = lane >> 4;
  const __amdgpu_buffer_rsrc_t rc = __builtin_amdgcn_make_buffer_rsrc((void*)((u16*)(p.ws + (mat ? WS_V : WS_K)) + (size_t)hf * 128 * HD + n0), 0, 0x7FFFFFFF, 0x00020000);
#pragma unroll
  for (int m = 0; m < 4; ++m)
    __builtin_amdgcn_raw_buffer_store_b128(u32x4{cvt_pk(acc[m][0][0], acc[m][0][1]), cvt_pk(acc[m][0][2], acc[m][0][3]), cvt_pk(acc[m][1][0], acc[m][1][1]), cvt_pk(acc[m][1][2], acc[m][1][3])},
                                           rc, ((wr * 64 + m * 16 + fr) * HD + wc * 32 + fq * 8) * 2, 0, 16);
}
constexpr int LDS_BYTES = 128 * 1024 + 64;
DEV void wait_cnts(const int wv, u32* ctl, int ia, int na, u32 ta, int ib, int nb, u32 tb) {
  if (wv == 0) {
    const int t = tid_of(0);
    u32* c = ctl + (t < na ? ia + t : (t < na + nb ? ib + t - na : ia));
    const u32 target = t < na ? ta : (t < na + nb ? tb : 0u);
    for (int sp = 0;; ++sp) {
      if (__all(ctl_ld(c) >= target)) break;
      __builtin_amdgcn_s_sleep(6);
      if (sp > 400000 || ((sp & 63) == 63 && __builtin_amdgcn_readfirstlane(ctl_ld(ctl + C_TMO)))) { ctl_st(ctl + C_TMO, 1); break; }
    }
  }
}
DEV void acquire_wg(const int wv) {
  if (tid_of(wv) == 0) { __builtin_amdgcn_fence(__ATOMIC_ACQUIRE, "agent"); asm volatile("s_waitcnt vmcnt(0)" ::: "memory"); }
  __syncthreads();
}
DEV void signal_wg(const int wv, u32* c) {
  asm volatile("s_waitcnt vmcnt(0)" ::: "memory");
  __syncthreads();
  if (tid_of(wv) == 0) ctl_add(c, 1);
}
#define NBIG 0
constexpr int N_KV0 = 128, N_KV = N_KV0 + 224, N_F1 = 112, N_Q = 128, N_Y = 64, N_G = 16 * 48, I_F1 = N_KV, I_Q = I_F1 + N_F1, I_Y = I_Q + N_Q, I_G = I_Y + N_Y, N_ITEMS = I_G + N_G;
typedef __attribute__((address_space(4))) const Params* KArg;
DEV void worker(const int wv, const int role, char* smem) {
  volatile int* s_idx = (volatile int*)(smem + 128 * 1024 + 16);
  int qn = 4, tried = 0;
  for (;;) {
    KArg ka = (KArg)__builtin_amdgcn_kernarg_segment_ptr(); asm volatile("" : "+s"(ka));
    Params p; p.x = ka->x; p.mem0 = ka->mem0; p.Wq = ka->Wq; p.Wk = ka->Wk; p.Wv = ka->Wv; p.Wo = ka->Wo; p.w1 = ka->w1; p.b1 = ka->b1; p.w2 = ka->w2; p.out = ka->out; p.ws = ka->ws;
    u32* ctl = (u32*)(p.ws + WS_CTL);
    const u16* XN = (const u16*)(p.ws + WS_XN);
    __syncthreads();
    if (tid_of(wv) == 0) *s_idx = (int)ctl_add(ctl + C_QUEUE + qn * 32, 1);
    __syncthreads();
    int idx = __builtin_amdgcn_readfirstlane(*s_idx);
    if (qn == 4) { if (idx >= I_G) { qn = (role >> 5) & 3; continue; } }
    else {
      if (idx >= 200) { if (++tried == 4) break; qn = (qn + 1) & 3; continue; }
      int g, k;
      if (idx < 4) { g = 0; k = idx; } else { const int u = idx - 4, gg = u / 12, kk = u - gg * 12;
        if (gg < 15) { if (kk < 4) { g = gg + 1; k = kk; } else { g = gg; k = kk; } } else { g = 15; k = u - 180 + 4; } }
      if (g < 2 * NBIG && k >= 4) {
        if (!(g & 1) || k != 4) continue;
        idx = I_G + N_G + 64 + (g >> 1) * 4 + qn;
      } else
      idx = (g == 15 && k >= 4) ? I_G + N_G + qn * 16 + (k - 4) : I_G + g * 48 + (k < 4 ? k * 4 + qn : 16 + qn * 8 + (k - 4));
    }
    if (idx < N_KV0) {
      kv0_item(wv, p, idx, smem);
      signal_wg(wv, ctl + C_KV);
    } else if (idx < I_G || idx >= I_G + N_G + 64) {
      const u16 *A, *B, *bias; void* C; int lda, ldb, K, ldc, mode; u32* done;
      if (idx < I_F1) { const int jj = idx - N_KV0 + 32, mt = jj >> 5, mat = (jj >> 4) & 1, n0 = (jj & 15) * 256;
        A = XN + (size_t)mt * 256 * D_; lda = D_; B = (mat ? p.Wv : p.Wk) + n0; ldb = HD; K = D_; C = (u16*)(p.ws + (mat ? WS_V : WS_K)) + (size_t)mt * 256 * HD + n0; ldc = HD; bias = p.b1; mode = 0; done = ctl + C_KV + mt * 32;
      } else if (idx < I_Q) { const int j = idx - I_F1, mt = j & 7, n0 = (j >> 3) * 256;
        A = XN + (size_t)mt * 256 * D_; lda = D_; B = p.w1 + n0; ldb = FF; K = D_; C = (u16*)(p.ws + WS_ACT) + (size_t)mt * 256 * FF + n0; ldc = FF; bias = p.b1 + n0; mode = 1; done = ctl + C_F1 + mt * 32;
      } else if (idx < I_Y) { const int j = idx - I_Q, mt = j >> 4, n0 = (j & 15) * 256;
        A = XN + (size_t)mt * 256 * D_; lda = D_; B = p.Wq + n0; ldb = HD; K = D_; C = (u16*)(p.ws + WS_Q) + (size_t)mt * 256 * HD + n0; ldc = HD; bias = p.b1; mode = 0; done = ctl + C_Q + j;
      } else if (idx >= I_G) { const int j = idx - (I_G + N_G + 64), mt = j >> 2, n0 = (j & 3) * 256;
        wait_cnts(wv, ctl, C_A + mt * 8, 8, 4, C_Y + mt * 4 + (j & 3), 1, 2); acquire_wg(wv);
        A = (const u16*)(p.ws + WS_O) + (size_t)mt * 256 * HD; lda = HD; B = p.Wo + n0; ldb = D_; K = HD; C = p.out + (size_t)mt * 256 * D_ + n0; ldc = D_;
        bias = (const u16*)((const float*)(p.ws + WS_Y) + (size_t)mt * 256 * D_ + n0); mode = 4; done = ctl + C_TMO + 1;
      } else { const int j = idx - I_Y, kh = j & 1, mt = j >> 3, n0 = ((j >> 1) & 3) * 256;
        wait_cnts(wv, ctl, C_F1 + mt * 32, 1, 14, 0, 0, 0); acquire_wg(wv);
        A = (const u16*)(p.ws + WS_ACT) + (size_t)mt * 256 * FF + kh * 1792; lda = FF; B = p.w2 + (size_t)kh * 1792 * D_ + n0; ldb = D_; K = 1792;
        C = (float*)(p.ws + WS_Y + (size_t)kh * (8u << 20)) + (size_t)mt * 256 * D_ + n0; ldc = D_; bias = p.x + (size_t)mt * 256 * D_ + n0; mode = kh ? 3 : 2; done = ctl + C_Y + (j >> 1);
      }
      const int dn = __builtin_amdgcn_readfirstlane((int)(done - ctl));
      gemm256(wv, __builtin_amdgcn_readfirstlane(mode), A, __builtin_amdgcn_readfirstlane(lda), B, __builtin_amdgcn_readfirstlane(ldb), __builtin_amdgcn_readfirstlane(K), C, __builtin_amdgcn_readfirstlane(ldc), uni(bias), smem);
      KArg kb = (KArg)__builtin_amdgcn_kernarg_segment_ptr(); asm volatile("" : "+s"(kb));
      signal_wg(wv, (u32*)(kb->ws + WS_CTL) + dn);
    } else if (idx >= I_G + N_G) {
      const int j = idx - I_G - N_G, kq = j >> 4, nt_ = j & 7, hf = (j >> 3) & 1;
      wait_cnts(wv, ctl, C_A + 60 + kq, 1, 4, 0, 0, 0);
      __syncthreads();
      wo_item<64>(wv, p, 30 + hf, nt_, kq, smem);
    } else {
      const int j = idx - I_G, g = j / 48, k = j - g * 48;
      if (k < 16) {
        const int seg = 4 * g + (k >> 2), h0 = 8 * (k & 3);
        wait_cnts(wv, ctl, C_Q + (seg >> 3) * 16 + (h0 >> 1), 4, 1, C_KV + (seg >> 3) * 32, 1, seg < 8 ? 128 : 32);
        __syncthreads();
        attn_item(wv, p, seg, h0 + wv, smem + wv * 12288);
        signal_wg(wv, ctl + C_A + g * 4 + (k & 3));
      } else {
        const int kq = (k - 16) >> 3, nt_ = (k - 16) & 7;
        wait_cnts(wv, ctl, C_A + g * 4 + kq, 1, 4, 0, 0, 0);
        __syncthreads();
        wo_item<128>(wv, p, g, nt_, kq, smem);
      }
    }
  }
}
__global__ void __launch_bounds__(512, 2) k_main(Params p) {
  extern __shared__ __attribute__((aligned(16))) char smem[];
  const int wv = __builtin_amdgcn_readfirstlane(threadIdx.x >> 6);
  const int role = take_role((u32*)(p.ws + WS_CTL), (int*)(smem + 128 * 1024));
  if (role >= 0 && role < 128) chain_role(p, role, smem);
  __syncthreads();
  worker(wv, role, smem);
}
extern "C" void kernel_launch(void* const* d_in, const int* in_sizes, int n_in, void* d_out, int out_size, void* d_ws, size_t ws_size, hipStream_t stream) {
  Params p;
  p.x = (const u16*)d_in[0]; p.mem0 = (const u16*)d_in[1]; p.Wq = (const u16*)d_in[2]; p.Wk = (const u16*)d_in[3]; p.Wv = (const u16*)d_in[4]; p.Wo = (const u16*)d_in[5];
  p.w1 = (const u16*)d_in[8]; p.b1 = (const u16*)d_in[9]; p.w2 = (const u16*)d_in[10]; p.out = (u16*)d_out; p.ws = (char*)d_ws;
  (void)hipFuncSetAttribute((const void*)k_main, hipFuncAttributeMaxDynamicSharedMemorySize, LDS_BYTES);
  k_prep<<<512, 256, 0, stream>>>(p);
  k_main<<<256, 512, LDS_BYTES, stream>>>(p);
}
```

```cpp
#include <hip/hip_runtime.h>
#include <hip/hip_bf16.h>
#include <stdint.h>

typedef unsigned int u32;
typedef unsigned short u16;
typedef u32 u32x4 __attribute__((ext_vector_type(4)));
typedef u32 u32x2 __attribute__((ext_vector_type(2)));
typedef short bf16x8 __attribute__((ext_vector_type(8)));
typedef short s16x4 __attribute__((ext_vector_type(4)));
typedef float f32x4 __attribute__((ext_vector_type(4)));
typedef __bf16 bf2_t __attribute__((ext_vector_type(2)));
typedef float f32x2 __attribute__((ext_vector_type(2)));
#define LDSP __attribute__((address_space(3)))
#define DEV __device__ __forceinline__

constexpr int S_ = 2048, D_ = 1024, HD = 4096, FF = 3584, NSEG = 64;
constexpr u32 SENT = 0xFFFFFFFFu;
constexpr size_t WS_XN = 0;
constexpr size_t WS_Q = 4u << 20;
constexpr size_t WS_K = 20u << 20;
constexpr size_t WS_V = 36u << 20;
constexpr size_t WS_ACT = 52u << 20;
constexpr size_t WS_O = 68u << 20;
constexpr size_t WS_KM = 84u << 20;
constexpr size_t WS_VM = 88u << 20;
constexpr size_t WS_CTL = 92u << 20;
constexpr size_t WS_XCH = 96u << 20;
constexpr size_t WS_Y = 100u << 20;
constexpr size_t WS_P = 116u << 20;
constexpr u32 SLOT = 256 * 1024, X_M = 0, X_O = 32768, X_SC = 98304;
constexpr int C_TICK = 0;
constexpr int C_QUEUE = 1408  , C_F1 = 512, C_Q = C_F1 + 8 * 32, C_Y = C_Q + 128, C_A = C_Y + 32  , C_P = C_A + 128  , C_KV = 1600  , C_KM = 2048;
constexpr int C_OVF = 8 * 32, C_TOTAL = 9 * 32, C_NEAR = 10 * 32, C_TMO = 11 * 32;

DEV u32 cvt_pk(float a, float b) { f32x2 v = {a, b}; bf2_t r = __builtin_convertvector(v, bf2_t); return __builtin_bit_cast(u32, r); }
DEV float bf_lo(u32 x) { return __uint_as_float(x << 16); }
DEV float bf_hi(u32 x) { return __uint_as_float(x & 0xFFFF0000u); }
DEV u32 nosent(u32 x) { return x == SENT ? 0xFFFEFFFFu : x; }
DEV float ror8(float x) { return __int_as_float(__builtin_amdgcn_update_dpp(0, __float_as_int(x), 0x128, 0xF, 0xF, true)); }
DEV u32 ror8u(u32 x) { return (u32)__builtin_amdgcn_update_dpp(0, (int)x, 0x128, 0xF, 0xF, true); }
DEV float xor16(float x) { u32 a = __float_as_uint(x); auto r = __builtin_amdgcn_permlane16_swap(a, a, false, false); return __uint_as_float((threadIdx.x & 16) ? r[0] : r[1]); }
DEV float xor32(float x) { u32 a = __float_as_uint(x); auto r = __builtin_amdgcn_permlane32_swap(a, a, false, false); return __uint_as_float((threadIdx.x & 32) ? r[0] : r[1]); }
DEV f32x4 mfma16(bf16x8 a, bf16x8 b, f32x4 c) { return __builtin_amdgcn_mfma_f32_16x16x32_bf16(a, b, c, 0, 0, 0); }
DEV bf16x8 asfrag(u32x4 v) { return __builtin_bit_cast(bf16x8, v); }
DEV s16x4 trread(const char* p) { return __builtin_amdgcn_ds_read_tr16_b64_v4i16((LDSP s16x4*)p); }
DEV bf16x8 cat4(s16x4 a, s16x4 b) { return __builtin_shufflevector(a, b, 0, 1, 2, 3, 4, 5, 6, 7); }
DEV bool valid4(u32x4 v) { return (v.x != SENT) & (v.y != SENT) & (v.z != SENT) & (v.w != SENT); }
DEV u32x4 ld_sc1(__amdgpu_buffer_rsrc_t rs, u32 off) { return __builtin_amdgcn_raw_buffer_load_b128(rs, off, 0, 16); }
DEV void st_sc1(__amdgpu_buffer_rsrc_t rs, u32 off, u32x4 v) { __builtin_amdgcn_raw_buffer_store_b128(v, rs, off, 0, 16); }
DEV u32 ctl_ld(u32* p) { return __hip_atomic_load(p, __ATOMIC_RELAXED, __HIP_MEMORY_SCOPE_AGENT); }
DEV void ctl_st(u32* p, u32 v) { __hip_atomic_store(p, v, __ATOMIC_RELAXED, __HIP_MEMORY_SCOPE_AGENT); }
DEV u32 ctl_add(u32* p, u32 v) { return __hip_atomic_fetch_add(p, v, __ATOMIC_RELAXED, __HIP_MEMORY_SCOPE_AGENT); }
DEV char* xch_base(char* ws) { return (char*)(((size_t)(ws + WS_XCH) + (2u << 20) - 1) & ~(size_t)((2u << 20) - 1)); }

DEV int tid_of(int wv) { int ln; asm volatile("v_mbcnt_lo_u32_b32 %0, -1, 0\n v_mbcnt_hi_u32_b32 %0, -1, %0" : "=v"(ln)); return wv * 64 + ln; }
template <class T> DEV T* uni(T* p) {
  const unsigned long long a = (unsigned long long)p;
  return (T*)(((unsigned long long)(u32)__builtin_amdgcn_readfirstlane((int)(a >> 32)) << 32) | (u32)__builtin_amdgcn_readfirstlane((int)a));
}

struct Params {
  const u16* x; const u16* mem0; const u16* Wq; const u16* Wk; const u16* Wv; const u16* Wo;
  const u16* w1; const u16* b1; const u16* w2; u16* out; char* ws;
};

__global__ void __launch_bounds__(256) k_prep(Params p) {
  const int tid = threadIdx.x, lane = tid & 63, wave = tid >> 6, b = blockIdx.x;
  {
    const int row = b * 4 + wave;
    const u32x4* src = (const u32x4*)(p.x + (size_t)row * D_);
    u32x4 v0 = src[lane], v1 = src[lane + 64];
    float f[16];
    f[0] = bf_lo(v0.x); f[1] = bf_hi(v0.x); f[2] = bf_lo(v0.y); f[3] = bf_hi(v0.y); f[4] = bf_lo(v0.z); f[5] = bf_hi(v0.z); f[6] = bf_lo(v0.w); f[7] = bf_hi(v0.w);
    f[8] = bf_lo(v1.x); f[9] = bf_hi(v1.x); f[10] = bf_lo(v1.y); f[11] = bf_hi(v1.y); f[12] = bf_lo(v1.z); f[13] = bf_hi(v1.z); f[14] = bf_lo(v1.w); f[15] = bf_hi(v1.w);
    float s = 0; for (int i = 0; i < 16; ++i) s += f[i];
    for (int o = 32; o; o >>= 1) s += __shfl_xor(s, o);
    const float mean = s * (1.f / D_);
    float q = 0; for (int i = 0; i < 16; ++i) { f[i] -= mean; q += f[i] * f[i]; }
    for (int o = 32; o; o >>= 1) q += __shfl_xor(q, o);
    const float rs = 1.f / sqrtf(q * (1.f / D_) + 1e-5f);
    u32x4 o0 = {cvt_pk(f[0] * rs, f[1] * rs), cvt_pk(f[2] * rs, f[3] * rs), cvt_pk(f[4] * rs, f[5] * rs), cvt_pk(f[6] * rs, f[7] * rs)};
    u32x4 o1 = {cvt_pk(f[8] * rs, f[9] * rs), cvt_pk(f[10] * rs, f[11] * rs), cvt_pk(f[12] * rs, f[13] * rs), cvt_pk(f[14] * rs, f[15] * rs)};
    u32x4* dst = (u32x4*)(p.ws + WS_XN + (size_t)row * D_ * 2);
    dst[lane] = o0; dst[lane + 64] = o1;
  }
  {
    const u32 off = (u32)(b * 256 + tid) * 16;
    const u32 x = ((off >> 14) << 13) | (off & 8191);
    u32x4 v = {SENT, SENT, SENT, SENT};
    if (x < 32768) {
      const u32 c8 = x >> 8, g = (x >> 7) & 1, r = (x >> 4) & 7;
      if (g == 0) { v = *(const u32x4*)(p.mem0 + r * D_ + c8 * 8); v.x = nosent(v.x); v.y = nosent(v.y); v.z = nosent(v.z); v.w = nosent(v.w); }
      else v = u32x4{0, 0, 0, 0};
    }
    *(u32x4*)(xch_base(p.ws) + off) = v;
  }
  if (b == 0) { u32* ctl = (u32*)(p.ws + WS_CTL); for (int i = tid; i < 4096; i += 256) ctl[i] = 0; }
}

DEV int take_role(u32* ctl, int* s_tmp) {
  if (threadIdx.x == 0) {
    u32 xcc; asm volatile("s_getreg_b32 %0, hwreg(HW_REG_XCC_ID)" : "=s"(xcc)); xcc &= 7;
    const u32 t = ctl_add(ctl + C_TICK + xcc * 32, 1);
    int role;
    if (t < 32) { role = xcc * 32 + t; ctl_add(ctl + C_TOTAL, 1); }
    else {
      const u32 k = ctl_add(ctl + C_OVF, 1); ctl_add(ctl + C_TOTAL, 1);
      int sp = 0; while (ctl_ld(ctl + C_TOTAL) < gridDim.x && ++sp < 2000000) __builtin_amdgcn_s_sleep(2);
      role = -1; u32 n = 0;
      for (int xx = 0; xx < 8; ++xx) { const u32 c = ctl_ld(ctl + C_TICK + xx * 32); if (c < 32) { if (k < n + (32 - c)) { role = xx * 32 + c + (k - n); break; } n += 32 - c; } }
    }
    *s_tmp = role;
  }
  __syncthreads();
  return __builtin_amdgcn_readfirstlane(*s_tmp);
}
DEV float red16_add(float x) { u32 a = __float_as_uint(x); auto r = __builtin_amdgcn_permlane16_swap(a, a, false, false); return __uint_as_float(r[0]) + __uint_as_float(r[1]); }
DEV float red32_add(float x) { u32 a = __float_as_uint(x); auto r = __builtin_amdgcn_permlane32_swap(a, a, false, false); return __uint_as_float(r[0]) + __uint_as_float(r[1]); }
DEV float red16_max(float x) { u32 a = __float_as_uint(x); auto r = __builtin_amdgcn_permlane16_swap(a, a, false, false); return fmaxf(__uint_as_float(r[0]), __uint_as_float(r[1])); }
DEV float red32_max(float x) { u32 a = __float_as_uint(x); auto r = __builtin_amdgcn_permlane32_swap(a, a, false, false); return fmaxf(__uint_as_float(r[0]), __uint_as_float(r[1])); }
DEV f32x4 halfsum4(f32x4 a, f32x4 b) {
  float d0, d1, d2, d3;
  asm volatile("s_nop 7\n s_nop 3\n"
               "v_add_f32_dpp %0, %4, %4 row_ror:8 row_mask:0xf bank_mask:0x3\n v_add_f32_dpp %1, %5, %5 row_ror:8 row_mask:0xf bank_mask:0x3\n"
               "v_add_f32_dpp %2, %6, %6 row_ror:8 row_mask:0xf bank_mask:0x3\n v_add_f32_dpp %3, %7, %7 row_ror:8 row_mask:0xf bank_mask:0x3\n"
               "v_add_f32_dpp %0, %8, %8 row_ror:8 row_mask:0xf bank_mask:0xc\n v_add_f32_dpp %1, %9, %9 row_ror:8 row_mask:0xf bank_mask:0xc\n"
               "v_add_f32_dpp %2, %10, %10 row_ror:8 row_mask:0xf bank_mask:0xc\n v_add_f32_dpp %3, %11, %11 row_ror:8 row_mask:0xf bank_mask:0xc\n s_nop 1"
               : "=&v"(d0), "=&v"(d1), "=&v"(d2), "=&v"(d3)
               : "v"(a[0]), "v"(a[1]), "v"(a[2]), "v"(a[3]), "v"(b[0]), "v"(b[1]), "v"(b[2]), "v"(b[3]));
  return f32x4{d0, d1, d2, d3};
}
DEV u32 nos(u32 x) { return min(x, 0xFFFFFFFEu); }
#define BARL() asm volatile("s_waitcnt lgkmcnt(0)\n s_barrier" ::: "memory")
DEV void nap(int n) { for (int i = 0; i < n; ++i) __builtin_amdgcn_s_sleep(1); }
#define CH_D1 12
#define CH_D3 10
#define CH_D2 8
#define SMW 2
#define CH_INC 1
constexpr int SPIN_CAP = 150000;
constexpr int L_RQK = 0;
constexpr int L_RV = 8 * 584 * 4;
constexpr int L_R2 = L_RV + 8 * 296 * 4;
constexpr int L_KT = L_R2 + 8 * 160 * 4;
constexpr int L_VT = L_KT + 2 * 32 * 64;
constexpr int L_VM = L_VT + 2 * 32 * 64;
constexpr int L_F = L_VM + 16 * 64 + 64;
constexpr int L_W2 = L_F + 1024;
constexpr int L_CHAIN_END = L_VM + 16 * 64;

#define POLL(N, V, LDEXPR, NSP) do { \
    if (dead) { _Pragma("unroll") for (int k = 0; k < N; ++k) V[k] = u32x4{0, 0, 0, 0}; } \
    else for (int sp = 0;; ++sp) { \
      asm volatile("" ::: "memory"); \
      _Pragma("unroll") for (int k = 0; k < N; ++k) V[k] = (LDEXPR); \
      u32 mxv = 0; \
      _Pragma("unroll") for (int k = 0; k < N; ++k) mxv = max(max(mxv, max(V[k].x, V[k].y)), max(V[k].z, V[k].w)); \
      if (__all(mxv != SENT)) { NSP += sp; break; } \
      if (sp > SPIN_CAP || ((sp & 255) == 255 && __builtin_amdgcn_readfirstlane(ctl_ld(ctl + C_TMO)))) { ctl_st(ctl + C_TMO, 1); dead = true; break; } \
    } } while (0)
DEV u32x4 ldx(__amdgpu_buffer_rsrc_t rs, u32 voff, u32 soff) { return __builtin_amdgcn_raw_buffer_load_b128(rs, voff, soff, 16); }
DEV void stx(__amdgpu_buffer_rsrc_t rs, u32 voff, u32 soff, u32x4 v) { __builtin_amdgcn_raw_buffer_store_b128(v, rs, voff, soff, 16); }

DEV void chain_role(const Params& p, const int role, char* smem) {
  const int tid = threadIdx.x, w = __builtin_amdgcn_readfirstlane(tid >> 6), l = tid & 63, q = l >> 4, i = l & 15, r = l & 7, g2 = (l >> 3) & 1;
  const int h = role >> 2, j = role & 3, cs = role >> 1, g = role & 1;
  u32* ctl = (u32*)(p.ws + WS_CTL);
  char* xb = xch_base(p.ws);
  if (role == 0 && w == 0) {
    u32 lat[2] = {1u << 30, 1u << 30};
    for (int rep = 0; rep < 24; ++rep) {
      const int c = rep & 1;
      u32* pq = (u32*)(xb + 127 * 16384 + c * 8192 + 8064) + (l & 31);
      const long long t0 = clock64();
      asm volatile("global_store_dword %0, %1, off sc1\n s_waitcnt vmcnt(0)" ::"v"(pq), "v"(SENT) : "memory");
      const u32 dt = (u32)(clock64() - t0);
      if (c) lat[1] = min(lat[1], dt); else lat[0] = min(lat[0], dt);
    }
    if (l == 0) ctl_st(ctl + C_NEAR, lat[1] < lat[0] ? 2u : 1u);
  }
  bf16x8 W1[6][4];
  bf16x8 W2t[8];
  {
    char* stg = smem + w * 8192;
    const u16* mats[3] = {p.Wq, p.Wk, p.Wv};
    u32x4 raw[4][8];
#pragma unroll
    for (int m = 0; m < 3; ++m) {
      const u16* src = mats[m] + (size_t)(128 * w) * HD + h * 128 + 32 * j;
#pragma unroll
      for (int it = 0; it < 8; ++it) raw[m][it] = *(const u32x4*)(src + (size_t)(it * 16 + (l >> 2)) * HD + (l & 3) * 8);
    }
    {
      const u16* src = p.Wo + (size_t)(2048 * g + 256 * w) * D_ + 16 * cs;
#pragma unroll
      for (int it = 0; it < 8; ++it) raw[3][it] = *(const u32x4*)(src + (size_t)(it * 32 + (l >> 1)) * D_ + (l & 1) * 8);
    }
    __builtin_amdgcn_sched_barrier(0);
#pragma unroll
    for (int m = 0; m < 3; ++m) {
#pragma unroll
      for (int it = 0; it < 8; ++it) *(u32x4*)(stg + (it * 16 + (l >> 2)) * 64 + (l & 3) * 16) = raw[m][it];
      asm volatile("s_waitcnt lgkmcnt(0)" ::: "memory");
#pragma unroll
      for (int t = 0; t < 2; ++t)
#pragma unroll
        for (int ks = 0; ks < 4; ++ks) {
          const char* a = stg + (32 * ks + 8 * q + (i >> 2)) * 64 + t * 32 + (i & 3) * 8;
          W1[m * 2 + t][ks] = cat4(trread(a), trread(a + 4 * 64));
        }
      asm volatile("s_waitcnt lgkmcnt(0)" ::: "memory");
    }
#pragma unroll
    for (int it = 0; it < 8; ++it) *(u32x4*)(stg + (it * 32 + (l >> 1)) * 32 + (l & 1) * 16) = raw[3][it];
    asm volatile("s_waitcnt lgkmcnt(0)" ::: "memory");
#pragma unroll
    for (int ks = 0; ks < 8; ++ks) { const char* a = stg + (32 * ks + 8 * q + (i >> 2)) * 32 + (i & 3) * 8; W2t[ks] = cat4(trread(a), trread(a + 4 * 32)); }
    asm volatile("s_waitcnt lgkmcnt(0)" ::: "memory");
  }
  __syncthreads();
#pragma unroll
  for (int ks = 0; ks < 8; ++ks) *(bf16x8*)(smem + L_W2 + (w * 8 + ks) * 1024 + l * 16) = W2t[ks];
  const unsigned long long kvb = (unsigned long long)uni((const u16*)(p.ws + WS_K) + h * 128 + 32 * j);
  const u32x4 rkv = {(u32)__builtin_amdgcn_readfirstlane((int)kvb), (u32)__builtin_amdgcn_readfirstlane((int)(kvb >> 32)), 0x7FFFFFFFu, 0x00020000u};
  const u32 vkv = (u32)((l >> 2) * HD + (l & 3) * 8) * 2u;
  const u32 ldsk = (u32)(size_t)(LDSP char*)(smem + L_KT);
  auto dma = [&](u32 dst, u32 so) { asm volatile("s_mov_b32 m0, %0\n s_nop 0\n buffer_load_dwordx4 %1, %2, %3 offen sc1 lds" ::"s"(dst), "v"(vkv), "s"(rkv), "s"(so) : "memory"); };
  auto prefetch = [&](int s) {
    if ((s & 7) == 0) {
      u32* c = ctl + C_KV + (s >> 3) * 32;
      for (int sp = 0; __builtin_amdgcn_readfirstlane(ctl_ld(c)) < (s ? 32u : 128u); ++sp) { __builtin_amdgcn_s_sleep(4); if (sp > 1000000) { ctl_st(ctl + C_TMO, 1); break; } }
    }
    const u32 d = ldsk + (s & 1) * 2048, so = (u32)s * 32 * HD * 2;
    dma(d, so); dma(d + 1024, so + 16 * HD * 2); dma(d + 4096, so + (u32)(WS_V - WS_K)); dma(d + 4096 + 1024, so + (u32)(WS_V - WS_K) + 16 * HD * 2);
  };
  if (w == 7) { prefetch(0); asm volatile("s_waitcnt vmcnt(0)" ::: "memory"); }
  if (w == 0 && l < 32) *(u32x4*)(smem + L_VM + 512 + l * 16) = u32x4{0, 0, 0, 0};
  if (tid == 0) { int sp = 0; u32 c; while ((c = ctl_ld(ctl + C_NEAR)) == 0 && ++sp < 4000000) __builtin_amdgcn_s_sleep(1); *(volatile u32*)(smem + L_CHAIN_END) = c; }
  __syncthreads();
  const u32 near = (__builtin_amdgcn_readfirstlane(*(volatile u32*)(smem + L_CHAIN_END)) == 2) ? 8192u : 0u;
  const __amdgpu_buffer_rsrc_t rs = __builtin_amdgcn_make_buffer_rsrc((void*)(xb + near), 0, 0x7FFFFFFF, 0x00020000);
  auto PH = [](u32 x) -> u32 { return ((x >> 13) << 14) | (x & 8191); };
  constexpr u32 X_S0 = X_SC + 131072;
  const u32 oM = PH(X_M + 4 * w * 1024 + l * 16), oO = PH(X_O + g * 32768 + (32 * w + 4 * g2 + q) * 128 + r * 16);
  const u32 oS = PH(X_SC + h * 4096 + g2 * 512 + q * 128 + r * 16);
  const u32 oS0 = PH(X_S0 + h * 1024 + ((q >> 1) * 2 + g2) * 256 + (q & 1) * 128 + r * 16);
  const u32 pS0 = PH(X_S0 + h * 1024 + j * 256 + (q & 1) * 128 + r * 16);
  const u32 pO = PH(X_O + role * 512 + q * 128 + r * 16);
  const u32 pM = PH(X_M + (2 * cs + ((l >> 2) & 1)) * 256 + g * 128 + (l >> 3) * 16);
  const u32 jo = j * 1024;
  float* RQK = (float*)(smem + L_RQK); float* RV = (float*)(smem + L_RV); float* R2 = (float*)(smem + L_R2);
  const __amdgpu_buffer_rsrc_t rkm = __builtin_amdgcn_make_buffer_rsrc((void*)((u16*)(p.ws + WS_KM) + h * 128 + 32 * j), 0, 0x7FFFFFFF, 0x00020000);
  bool dead = false; int ns1 = 0, ns2 = 0, ns3 = 0;
  int d1 = (w == 1 ? CH_D1 - 4 : CH_D1), d2 = CH_D2, d3 = CH_D3;
  const u32x4 SV = {SENT, SENT, SENT, SENT};

  auto reset = [&](int s) {
    const u32 so = (u32)((s - 2) & 3) * (2 * SLOT);
    stx(rs, oS, so + jo, SV); if (l < 32 && g2 == 0) stx(rs, pS0, so, SV);
    if (g2 == 0) stx(rs, pO, so, SV);
    if ((l & 3) == 0) stx(rs, pM, so, SV);
  };
#define RESET() do { if (w == 6 && s >= 2) reset(s); } while (0)
#define PFPOS 3
#define RSPOS 0
  for (int s = 0; s < NSEG; ++s) {
    const u32 sb = (u32)(s & 3) * (2 * SLOT);
    u32x4 mv[4];
    if (s) nap(d1);
    f32x4 acc[6];
    const f32x4 z4 = {0, 0, 0, 0};
    {
#pragma unroll
      for (int k = 0; k < 4; ++k) mv[k] = ldx(rs, oM + k * 1024, sb);
      if (PFPOS == 4 && w == 7 && s + 1 < NSEG) prefetch(s + 1);
#pragma unroll
      for (int ks = 0; ks < 4; ++ks)
#pragma unroll
        for (int t = 0; t < 4; ++t) acc[t] = mfma16(W1[t][ks], asfrag(mv[ks]), ks ? acc[t] : z4);
      u32 mxv = 0;
#pragma unroll
      for (int k = 0; k < 4; ++k) mxv = max(max(mxv, max(mv[k].x, mv[k].y)), max(mv[k].z, mv[k].w));
      if (!__all(mxv != SENT)) {
        ++ns1; d1 = min(d1 + CH_INC, CH_D1 + 10);
        POLL(4, mv, ldx(rs, oM + k * 1024, sb), ns1);
#pragma unroll
        for (int ks = 0; ks < 4; ++ks)
#pragma unroll
          for (int t = 0; t < 4; ++t) acc[t] = mfma16(W1[t][ks], asfrag(mv[ks]), ks ? acc[t] : z4);
      }
    }
    {
      f32x4 a, b;
      a = halfsum4(acc[0], acc[2]); b = halfsum4(acc[1], acc[3]);
      float* d = RQK + w * 584 + g2 * 288 + r * 36 + 4 * q;
      *(f32x4*)d = a; *(f32x4*)(d + 16) = b;
    }
    BARL();
    if (PFPOS == 0 && w == 7 && s + 1 < NSEG) prefetch(s + 1);
    if (RSPOS == 0) RESET();
    {
      const float* b = RQK + (l >> 5) * 288 + w * 36 + (l & 31);
      float v[8];
#pragma unroll
      for (int ww = 0; ww < 8; ++ww) v[ww] = b[ww * 584];
      __builtin_amdgcn_sched_barrier(0);
      const float t = ((v[0] + v[1]) + (v[2] + v[3])) + ((v[4] + v[5]) + (v[6] + v[7]));
      const float o = __int_as_float(__builtin_amdgcn_update_dpp(0, __float_as_int(t), 0xB1, 0xF, 0xF, true));
      if ((l & 1) == 0) *(u32*)(smem + L_F + (l >> 5) * 512 + w * 64 + (l & 31) * 2) = cvt_pk(t, o);
    }
    BARL();
    if (PFPOS == 3 && w == 7 && s + 1 < NSEG) prefetch(s + 1);
    if (RSPOS == 3) RESET();
    if (w == 0) {
      const u32x4 qf = *(const u32x4*)(smem + L_F + r * 64 + q * 16);
      const u32x4 kfrag = *(const u32x4*)(smem + L_F + 512 + r * 64 + q * 16);
      const char* kt = smem + L_KT + (s & 1) * 2048 + i * 64 + q * 16;
      const bf16x8 kt0 = *(const bf16x8*)kt, kt1 = *(const bf16x8*)(kt + 1024);
      const f32x4 z = {0, 0, 0, 0};
      const f32x4 c1 = mfma16(kt0, asfrag(qf), z);
      const f32x4 c2 = mfma16(kt1, asfrag(qf), z);
      const f32x4 c0 = mfma16(asfrag(kfrag), asfrag(qf), z);
      if (s < NSEG - 1) {
        const u32x4 v12 = {nos(__float_as_uint(g2 ? c2[0] : c1[0])), nos(__float_as_uint(g2 ? c2[1] : c1[1])), nos(__float_as_uint(g2 ? c2[2] : c1[2])), nos(__float_as_uint(g2 ? c2[3] : c1[3]))};
        stx(rs, oS, sb + jo, v12);
        const u32x4 v0 = {nos(__float_as_uint(c0[0])), nos(__float_as_uint(c0[1])), nos(__float_as_uint(c0[2])), nos(__float_as_uint(c0[3]))};
        if (l < 32 && g2 == 0) stx(rs, pS0, sb, v0);
      }
      if (g2 == 0) __builtin_amdgcn_raw_buffer_store_b128(kfrag, rkm, (r * HD + 8 * q) * 2, s * 8 * HD * 2, 16);
    }
#pragma unroll
    for (int ks = 0; ks < 4; ++ks) { acc[4] = mfma16(W1[4][ks], asfrag(mv[ks]), ks ? acc[4] : z4); acc[5] = mfma16(W1[5][ks], asfrag(mv[ks]), ks ? acc[5] : z4); }
    {
      f32x4 a;
      a = halfsum4(acc[4], acc[5]);
      *(f32x4*)(RV + w * 296 + r * 36 + 16 * g2 + 4 * q) = a;
    }
    u32x4 sv[5];
    if (w == SMW && s < NSEG - 1) {
      nap(d2);
#pragma unroll
      for (int k = 0; k < 5; ++k) sv[k] = k < 4 ? ldx(rs, oS + k * 1024, sb) : ldx(rs, oS0, sb);
    }
    BARL();
    if (PFPOS == 2 && w == 7 && s + 1 < NSEG) prefetch(s + 1);
    if (RSPOS == 2) RESET();
    if (w == SMW) {
      f32x4 rd[4][2];
#pragma unroll
      for (int ww = 0; ww < 4; ++ww) { const float* b = RV + (4 * g2 + ww) * 296 + r * 36 + 8 * q; rd[ww][0] = *(const f32x4*)b; rd[ww][1] = *(const f32x4*)(b + 4); }
      __builtin_amdgcn_sched_barrier(0);
      f32x4 s0 = (rd[0][0] + rd[1][0]) + (rd[2][0] + rd[3][0]), s1 = (rd[0][1] + rd[1][1]) + (rd[2][1] + rd[3][1]);
#pragma unroll
      for (int e = 0; e < 4; ++e) { s0[e] += ror8(s0[e]); s1[e] += ror8(s1[e]); }
      const u32x4 vf = {cvt_pk(s0[0], s0[1]), cvt_pk(s0[2], s0[3]), cvt_pk(s1[0], s1[1]), cvt_pk(s1[2], s1[3])};
      if (g2 == 0) *(u32x4*)(smem + L_VM + r * 64 + q * 16) = vf;
      if (s < NSEG - 1) {
        const char* vt = smem + L_VT + (s & 1) * 2048 + (4 * q + (i >> 2)) * 64 + (i & 3) * 16;
        const char* vm = smem + L_VM + (4 * q + (i >> 2)) * 64 + (i & 3) * 16;
        const bf16x8 vt0 = cat4(trread(vt), trread(vt + 1024)), vt1 = cat4(trread(vt + 8), trread(vt + 1024 + 8));
        const s16x4 zz = {0, 0, 0, 0};
        const bf16x8 vm0 = cat4(trread(vm), zz), vm1 = cat4(trread(vm + 8), zz);
        {
          u32 mxv = 0;
#pragma unroll
          for (int k = 0; k < 5; ++k) mxv = max(max(mxv, max(sv[k].x, sv[k].y)), max(sv[k].z, sv[k].w));
          if (!__all(mxv != SENT)) { ++ns2; d2 = min(d2 + CH_INC, CH_D2 + 10); POLL(5, sv, (k < 4 ? ldx(rs, oS + k * 1024, sb) : ldx(rs, oS0, sb)), ns2); }
        }
        const float sc = 0.08838834764831845f * 1.4426950408889634f;
        float st[4], sm[4];
        st[0] = (__uint_as_float(sv[0].x) + __uint_as_float(sv[1].x)) + (__uint_as_float(sv[2].x) + __uint_as_float(sv[3].x));
        st[1] = (__uint_as_float(sv[0].y) + __uint_as_float(sv[1].y)) + (__uint_as_float(sv[2].y) + __uint_as_float(sv[3].y));
        st[2] = (__uint_as_float(sv[0].z) + __uint_as_float(sv[1].z)) + (__uint_as_float(sv[2].z) + __uint_as_float(sv[3].z));
        st[3] = (__uint_as_float(sv[0].w) + __uint_as_float(sv[1].w)) + (__uint_as_float(sv[2].w) + __uint_as_float(sv[3].w));
        sm[0] = __uint_as_float(sv[4].x); sm[1] = __uint_as_float(sv[4].y); sm[2] = __uint_as_float(sv[4].z); sm[3] = __uint_as_float(sv[4].w);
#pragma unroll
        for (int e = 0; e < 4; ++e) sm[e] = red32_add(sm[e] + ror8(sm[e]));
        float mx = fmaxf(fmaxf(fmaxf(st[0], st[1]), fmaxf(st[2], st[3])), fmaxf(fmaxf(sm[0], sm[1]), fmaxf(sm[2], sm[3])));
        mx = fmaxf(mx, ror8(mx)); mx = red32_max(red16_max(mx));
        const float nm = -mx * sc;
#pragma unroll
        for (int e = 0; e < 4; ++e) { st[e] = __builtin_amdgcn_exp2f(fmaf(st[e], sc, nm)); sm[e] = __builtin_amdgcn_exp2f(fmaf(sm[e], sc, nm)); }
        const u32 p0 = cvt_pk(st[0], st[1]), p1 = cvt_pk(st[2], st[3]);
        const u32 m0 = cvt_pk(sm[0], sm[1]), m1 = cvt_pk(sm[2], sm[3]);
        const u32x4 pt = {p0, p1, ror8u(p0), ror8u(p1)};
        const u32x4 pm = {q < 2 ? m0 : 0u, q < 2 ? m1 : 0u, 0u, 0u};
        const f32x4 z = {0, 0, 0, 0};
        const f32x4 o0 = mfma16(vm0, asfrag(pm), mfma16(vt0, asfrag(pt), z)), o1 = mfma16(vm1, asfrag(pm), mfma16(vt1, asfrag(pt), z));
        float ts = (bf_lo(p0) + bf_hi(p0)) + (bf_lo(p1) + bf_hi(p1)), ms = (bf_lo(m0) + bf_hi(m0)) + (bf_lo(m1) + bf_hi(m1));
        ts += ror8(ts); ts = red32_add(red16_add(ts)); ms = red16_add(ms);
        const float inv = __builtin_amdgcn_rcpf(ts + ms);
        const u32x4 ov = {nos(cvt_pk(o0[0] * inv, o0[1] * inv)), nos(cvt_pk(o0[2] * inv, o0[3] * inv)), nos(cvt_pk(o1[0] * inv, o1[1] * inv)), nos(cvt_pk(o1[2] * inv, o1[3] * inv))};
        if (g2 == 0) stx(rs, pO, sb, ov);
      }
      if (g2 == 0) __builtin_amdgcn_raw_buffer_store_b128(vf, rkm, (4u << 20) + (r * HD + 8 * q) * 2, s * 8 * HD * 2, 16);
    }
    if (s == NSEG - 1) {
      asm volatile("s_waitcnt vmcnt(0)" ::: "memory");
      BARL();
      if (tid == 0) ctl_add(ctl + C_KM + s * 32 + h, 1);
      break;
    }
    BARL();
    if (PFPOS == 1 && w == 7) prefetch(s + 1);
    if (RSPOS == 1) RESET();
    u32x4 ov[4];
    bf16x8 W2[8];
#pragma unroll
    for (int ks = 0; ks < 8; ++ks) W2[ks] = *(const bf16x8*)(smem + L_W2 + (w * 8 + ks) * 1024 + l * 16);
    nap(d3);
    f32x4 a0, a1;
    {
#pragma unroll
      for (int k = 0; k < 4; ++k) ov[k] = ldx(rs, oO + k * 1024, sb);
#pragma unroll
      for (int k = 0; k < 4; ++k) { a0 = mfma16(W2[2 * k], asfrag(ov[k]), k ? a0 : z4); a1 = mfma16(W2[2 * k + 1], asfrag(ov[k]), k ? a1 : z4); }
      u32 mxv = 0;
#pragma unroll
      for (int k = 0; k < 4; ++k) mxv = max(max(mxv, max(ov[k].x, ov[k].y)), max(ov[k].z, ov[k].w));
      if (!__all(mxv != SENT)) {
        ++ns3; d3 = min(d3 + CH_INC, CH_D3 + 10);
        POLL(4, ov, ldx(rs, oO + k * 1024, sb), ns3);
#pragma unroll
        for (int k = 0; k < 4; ++k) { a0 = mfma16(W2[2 * k], asfrag(ov[k]), k ? a0 : z4); a1 = mfma16(W2[2 * k + 1], asfrag(ov[k]), k ? a1 : z4); }
      }
    }
    {
      f32x4 a;
#pragma unroll
      for (int e = 0; e < 4; ++e) a[e] = a0[e] + ror8(a1[e]);
      if (g2 == 0) *(f32x4*)(R2 + w * 160 + r * 20 + 4 * q) = a;
    }
    asm volatile("s_waitcnt vmcnt(0)" ::: "memory");
    BARL();
    if (tid == 192) ctl_add(ctl + C_KM + s * 32 + h, 1);
    if (w == 1) {
      const int n2 = l & 7, rr = l >> 3;
      f32x2 rr8[8];
#pragma unroll
      for (int ww = 0; ww < 8; ++ww) rr8[ww] = *(const f32x2*)(R2 + ww * 160 + rr * 20 + 2 * n2);
      __builtin_amdgcn_sched_barrier(0);
      const f32x2 sum = ((rr8[0] + rr8[1]) + (rr8[2] + rr8[3])) + ((rr8[4] + rr8[5]) + (rr8[6] + rr8[7]));
      const u32 v = nos(cvt_pk(sum[0], sum[1]));
      const u32x4 o = {(u32)__builtin_amdgcn_update_dpp(0, (int)v, 0x00, 0xF, 0xF, true), (u32)__builtin_amdgcn_update_dpp(0, (int)v, 0x55, 0xF, 0xF, true),
                       (u32)__builtin_amdgcn_update_dpp(0, (int)v, 0xAA, 0xF, 0xF, true), (u32)__builtin_amdgcn_update_dpp(0, (int)v, 0xFF, 0xF, 0xF, true)};
      if ((l & 3) == 0) stx(rs, pM, (u32)((s + 1) & 3) * (2 * SLOT), o);
    }
  }
}
constexpr int G_HT = 128 * 64;
DEV int lds_byte(int r, int c) { int st = (r >> 4) * 2 + (c >> 5), rr = r & 15, cc = c & 31, ob = rr * 64 + cc * 2; return st * 1024 + (ob ^ (((ob >> 9) & 1) << 5)); }
DEV void stage_rc(int b, int& R, int& C) { int st = b / 1024, sb = b % 1024, swz = sb ^ (((sb >> 9) & 1) << 5); R = (st >> 1) * 16 + swz / 64; C = (st & 1) * 32 + (swz % 64) / 2; }
DEV int swzb(int row) { return ((row & 3) << 2) | ((row >> 2) & 3); }
DEV float mish(float h) {
  const float u = __expf(-fabsf(h));
  const float n = h >= 0.f ? 1.f + 2.f * u : u * (u + 2.f);
  const float d = h >= 0.f ? n + 2.f * u * u : n + 2.f;
  return h * n * __builtin_amdgcn_rcpf(d);
}
DEV void gemm256(const int wv, const int MODE, const u16* __restrict__ A, const int lda, const u16* __restrict__ B, const int ldb, const int K, void* C, const int ldc, const u16* bias, char* smem) {
  u16* shm = (u16*)smem;
#define SA(b, h) (shm + ((b) * 2 + (h)) * G_HT)
#define SB(b, h) (shm + (4 + (b) * 2 + (h)) * G_HT)
#define STA_X(T, VO, P, br, kt) do { const int _s = ((br) * lda + (kt) * 64) * 2; \
    __builtin_amdgcn_raw_ptr_buffer_load_lds(rsa, (LDSP void*)((char*)(P) + (T) * 16), 16, (VO), _s, 0, 0); \
    __builtin_amdgcn_raw_ptr_buffer_load_lds(rsa, (LDSP void*)((char*)(P) + (T) * 16 + 8192), 16, (VO), _s + 128 * lda, 0, 0); } while (0)
#define STB_X(T, VO, P, bc, kt) do { const int _s = ((kt) * 64 * ldb + (bc)) * 2; \
    __builtin_amdgcn_raw_ptr_buffer_load_lds(rsb, (LDSP void*)((char*)(P) + (T) * 16), 16, (VO), _s, 0, 0); \
    __builtin_amdgcn_raw_ptr_buffer_load_lds(rsb, (LDSP void*)((char*)(P) + (T) * 16 + 8192), 16, (VO), _s + 64 * ldb, 0, 0); } while (0)
#define STA(P, br, kt) STA_X(t0, voa, P, br, kt)
#define STB(P, bc, kt) STB_X(t0, vob, P, bc, kt)
#define LDA(dst, b, h) for (int m = 0; m < 4; ++m) for (int k = 0; k < 2; ++k) dst[m][k] = *reinterpret_cast<const bf16x8*>((char*)SA(b, h) + lds_byte(wr * 64 + m * 16 + fr, k * 32 + fq * 8))
#define LDB(dst, b, h) for (int n = 0; n < 2; ++n) for (int k = 0; k < 2; ++k) { const char* _p = (const char*)SB(b, h) + k * 8192; \
      dst[n][k] = cat4(trread(_p + bo[n][0]), trread(_p + bo[n][1])); }
#define MMA(ai, bj, At, Bt) do { __builtin_amdgcn_s_setprio(1); \
    for (int m = 0; m < 4; ++m) for (int n = 0; n < 2; ++n) for (int k = 0; k < 2; ++k) acc[ai][bj][m][n] = mfma16(Bt[n][k], At[m][k], acc[ai][bj][m][n]); \
    __builtin_amdgcn_s_setprio(0); } while (0)
#define WAIT_V(n) asm volatile("s_waitcnt vmcnt(" #n ")" ::: "memory")
#define WAIT_L(n) asm volatile("s_waitcnt lgkmcnt(" #n ")" ::: "memory")
#define BAR __builtin_amdgcn_s_barrier()
#define SCHED __builtin_amdgcn_sched_barrier(0)
  const int t0 = tid_of(wv);
  int wid = t0 >> 6, lane = t0 & 63, wr = wid >> 2, wc = wid & 3, fr = lane & 15, fq = lane >> 4;
  f32x4 acc[2][2][4][2] = {};
  bf16x8 At[4][2], B0[2][2], B1[2][2];
  int bo[2][2];
  for (int n = 0; n < 2; ++n) for (int t = 0; t < 2; ++t) { const int row = 8 * fq + 4 * t + (fr >> 2), ch = 4 * wc + (fr & 3); bo[n][t] = 256 * row + 16 * (ch ^ swzb(row)) + 8 * n; }
  const int nt = K / 64;
  const __amdgpu_buffer_rsrc_t rsa = __builtin_amdgcn_make_buffer_rsrc((void*)uni(A), 0, 0x7FFFFFFF, 0x00020000), rsb = __builtin_amdgcn_make_buffer_rsrc((void*)uni(B), 0, 0x7FFFFFFF, 0x00020000);
  u32 voa, vob;
  { int _r, _c; stage_rc(t0 * 16, _r, _c); voa = (u32)(_r * lda + _c) * 2u; const int _b = t0 * 16, _rb = _b >> 8; vob = (u32)(_rb * ldb + ((((_b >> 4) & 15) ^ swzb(_rb)) * 8)) * 2u; }
  STB(SB(0, 0), 0, 0); STA(SA(0, 0), 0, 0); STB(SB(0, 1), 128, 0); STA(SA(0, 1), 128, 0);
  if (wr == 1) BAR;
  WAIT_V(4); BAR;
  STB(SB(1, 0), 0, 1); STA(SA(1, 0), 0, 1); STB(SB(1, 1), 128, 1);
  WAIT_V(6); BAR;
  for (int t = 0; t < nt - 2; t += 2) {
    LDB(B0, 0, 0); SCHED; LDA(At, 0, 0); STA(SA(1, 1), 128, t + 1);
    WAIT_L(8); BAR; WAIT_L(0); MMA(0, 0, At, B0); BAR; SCHED;
    LDB(B1, 0, 1); STB(SB(0, 0), 0, t + 2);
    BAR; WAIT_L(0); MMA(0, 1, At, B1); BAR;
    LDA(At, 0, 1); STA(SA(0, 0), 0, t + 2);
    BAR; WAIT_L(0); MMA(1, 0, At, B0); BAR; SCHED;
    STB(SB(0, 1), 128, t + 2);
    WAIT_V(6); BAR; MMA(1, 1, At, B1); BAR;
    LDB(B0, 1, 0); SCHED; LDA(At, 1, 0); STA(SA(0, 1), 128, t + 2);
    WAIT_L(8); BAR; WAIT_L(0); MMA(0, 0, At, B0); BAR; SCHED;
    LDB(B1, 1, 1); STB(SB(1, 0), 0, t + 3);
    BAR; WAIT_L(0); MMA(0, 1, At, B1); BAR;
    LDA(At, 1, 1); STA(SA(1, 0), 0, t + 3);
    BAR; WAIT_L(0); MMA(1, 0, At, B0); BAR; SCHED;
    STB(SB(1, 1), 128, t + 3);
    WAIT_V(6); BAR; MMA(1, 1, At, B1); BAR;
  }
  int tz = tid_of(wv); wid = tz >> 6; lane = tz & 63; wr = wid >> 2; wc = wid & 3; fr = lane & 15; fq = lane >> 4;
  { LDB(B0, 0, 0); WAIT_V(0); LDA(At, 0, 0); { int _r, _c; stage_rc(tz * 16, _r, _c); const u32 voa2 = (u32)(_r * lda + _c) * 2u; STA_X(tz, voa2, SA(1, 1), 128, nt - 1); }
    BAR; WAIT_L(0); MMA(0, 0, At, B0); BAR;
    LDB(B1, 0, 1); BAR; WAIT_L(0); MMA(0, 1, At, B1); BAR;
    LDA(At, 0, 1); WAIT_V(4); BAR; WAIT_L(0); MMA(1, 0, At, B0); MMA(1, 1, At, B1); BAR; }
  { LDB(B0, 1, 0); LDA(At, 1, 0); WAIT_V(2); BAR; WAIT_L(0); MMA(0, 0, At, B0); BAR;
    LDB(B1, 1, 1); WAIT_V(0); BAR; WAIT_L(0); MMA(0, 1, At, B1); BAR;
    LDA(At, 1, 1); BAR; WAIT_L(0); MMA(1, 0, At, B0); MMA(1, 1, At, B1); BAR; }
  if (wr == 0) BAR;
  const __amdgpu_buffer_rsrc_t rc = __builtin_amdgcn_make_buffer_rsrc((void*)uni((char*)C), 0, 0x7FFFFFFF, 0x00020000);
#pragma unroll
  for (int ai = 0; ai < 2; ++ai)
#pragma unroll
  for (int bj = 0; bj < 2; ++bj)
#pragma unroll
  for (int m = 0; m < 4; ++m) {
    const int row = ai * 128 + wr * 64 + m * 16 + fr, col = bj * 128 + wc * 32 + fq * 8;
    f32x4 v0 = acc[ai][bj][m][0], v1 = acc[ai][bj][m][1];
    if (MODE == 4) {
      const __amdgpu_buffer_rsrc_t ry = __builtin_amdgcn_make_buffer_rsrc((void*)bias, 0, 0x7FFFFFFF, 0x00020000);
      const u32 o = (u32)(row * ldc + col) * 4;
      v0 += __builtin_bit_cast(f32x4, __builtin_amdgcn_raw_buffer_load_b128(ry, o, 0, 16)) + __builtin_bit_cast(f32x4, __builtin_amdgcn_raw_buffer_load_b128(ry, o, 8u << 20, 16));
      v1 += __builtin_bit_cast(f32x4, __builtin_amdgcn_raw_buffer_load_b128(ry, o + 16, 0, 16)) + __builtin_bit_cast(f32x4, __builtin_amdgcn_raw_buffer_load_b128(ry, o + 16, 8u << 20, 16));
      __builtin_amdgcn_raw_buffer_store_b128(u32x4{cvt_pk(v0[0], v0[1]), cvt_pk(v0[2], v0[3]), cvt_pk(v1[0], v1[1]), cvt_pk(v1[2], v1[3])}, rc, (row * ldc + col) * 2, 0, 0);
    } else if (MODE >= 2) {
      if (MODE == 2) { const u32x4 xv = *(const u32x4*)(bias + (long)row * ldc + col);
        v0[0] += bf_lo(xv.x); v0[1] += bf_hi(xv.x); v0[2] += bf_lo(xv.y); v0[3] += bf_hi(xv.y); v1[0] += bf_lo(xv.z); v1[1] += bf_hi(xv.z); v1[2] += bf_lo(xv.w); v1[3] += bf_hi(xv.w); }
      __builtin_amdgcn_raw_buffer_store_b128(__builtin_bit_cast(u32x4, v0), rc, (row * ldc + col) * 4, 0, 16);
      __builtin_amdgcn_raw_buffer_store_b128(__builtin_bit_cast(u32x4, v1), rc, (row * ldc + col) * 4 + 16, 0, 16);
    } else {
      if (MODE == 1) { const u32x4 bb = *(const u32x4*)(bias + col);
        v0[0] = mish(v0[0] + bf_lo(bb.x)); v0[1] = mish(v0[1] + bf_hi(bb.x)); v0[2] = mish(v0[2] + bf_lo(bb.y)); v0[3] = mish(v0[3] + bf_hi(bb.y));
        v1[0] = mish(v1[0] + bf_lo(bb.z)); v1[1] = mish(v1[1] + bf_hi(bb.z)); v1[2] = mish(v1[2] + bf_lo(bb.w)); v1[3] = mish(v1[3] + bf_hi(bb.w)); }
      __builtin_amdgcn_raw_buffer_store_b128(u32x4{cvt_pk(v0[0], v0[1]), cvt_pk(v0[2], v0[3]), cvt_pk(v1[0], v1[1]), cvt_pk(v1[2], v1[3])}, rc, (row * ldc + col) * 2, 0, 16);
    }
  }
#undef SA
#undef SB
#undef LDA
#undef LDB
#undef MMA
}
DEV void wait_cnts(const int wv, u32* ctl, int ia, int na, u32 ta, int ib, int nb, u32 tb);
DEV void attn_item(const int wv, const Params& p, int s, int h, char* lds) {
  const int t0 = tid_of(0);
  const int l = t0 & 63, q = l >> 4, i = l & 15;
  const __amdgpu_buffer_rsrc_t rq = __builtin_amdgcn_make_buffer_rsrc((void*)((const u16*)(p.ws + WS_Q) + (size_t)s * 32 * HD + h * 128), 0, 0x7FFFFFFF, 0x00020000);
  const __amdgpu_buffer_rsrc_t rm = __builtin_amdgcn_make_buffer_rsrc((void*)((const u16*)(p.ws + WS_KM) + (size_t)s * 8 * HD + h * 128), 0, 0x7FFFFFFF, 0x00020000);
  const __amdgpu_buffer_rsrc_t rk = __builtin_amdgcn_make_buffer_rsrc((void*)((const u16*)(p.ws + WS_K) + (size_t)s * 32 * HD + h * 128), 0, 0x7FFFFFFF, 0x00020000);
  u32x4 vv[8]; bf16x8 qf[4][2], kf[4][2];
#pragma unroll
  for (int it = 0; it < 8; ++it) vv[it] = __builtin_amdgcn_raw_buffer_load_b128(rk, ((it * 4 + q) * HD + i * 8) * 2, (u32)(WS_V - WS_K), 16);
#pragma unroll
  for (int ks = 0; ks < 4; ++ks) {
    const int e = ks * 32 + q * 8;
    qf[ks][0] = asfrag(__builtin_amdgcn_raw_buffer_load_b128(rq, (i * HD + e) * 2, 0, 16)); qf[ks][1] = asfrag(__builtin_amdgcn_raw_buffer_load_b128(rq, ((16 + i) * HD + e) * 2, 0, 16));
    kf[ks][0] = asfrag(__builtin_amdgcn_raw_buffer_load_b128(rk, (i * HD + e) * 2, 0, 16)); kf[ks][1] = asfrag(__builtin_amdgcn_raw_buffer_load_b128(rk, ((16 + i) * HD + e) * 2, 0, 16));
  }
  __builtin_amdgcn_sched_barrier(0);
#pragma unroll
  for (int it = 0; it < 8; ++it) { const int row = 16 + it * 4 + q; *(u32x4*)(lds + row * 256 + 16 * (i ^ swzb(row))) = vv[it]; }
#pragma unroll
  for (int it = 2; it < 4; ++it) { const int row = it * 4 + q; *(u32x4*)(lds + row * 256 + 16 * (i ^ swzb(row))) = u32x4{0, 0, 0, 0}; }
  f32x4 c[3][2];
#pragma unroll
  for (int t = 0; t < 3; ++t) c[t][0] = c[t][1] = f32x4{0, 0, 0, 0};
#pragma unroll
  for (int ks = 0; ks < 4; ++ks) { c[1][0] = mfma16(kf[ks][0], qf[ks][0], c[1][0]); c[1][1] = mfma16(kf[ks][0], qf[ks][1], c[1][1]); c[2][1] = mfma16(kf[ks][1], qf[ks][1], c[2][1]); }
  wait_cnts(wv, (u32*)(p.ws + WS_CTL), C_KM + s * 32 + (h - wv), 8, 4, 0, 0, 0);
  __syncthreads();
  {
    u32x4 vm[2]; bf16x8 km[4];
#pragma unroll
    for (int it = 0; it < 2; ++it) vm[it] = __builtin_amdgcn_raw_buffer_load_b128(rm, (4u << 20) + ((it * 4 + q) * HD + i * 8) * 2, 0, 16);
#pragma unroll
    for (int ks = 0; ks < 4; ++ks) km[ks] = asfrag(__builtin_amdgcn_raw_buffer_load_b128(rm, ((i & 7) * HD + ks * 32 + q * 8) * 2, 0, 16));
#pragma unroll
    for (int it = 0; it < 2; ++it) { const int row = it * 4 + q; *(u32x4*)(lds + row * 256 + 16 * (i ^ swzb(row))) = vm[it]; }
#pragma unroll
    for (int ks = 0; ks < 4; ++ks) { c[0][0] = mfma16(km[ks], qf[ks][0], c[0][0]); c[0][1] = mfma16(km[ks], qf[ks][1], c[0][1]); }
  }
  const float sc = 0.08838834764831845f * 1.4426950408889634f, NEG = -3.0e38f;
  u32x4 pt[2], pm[2];
#pragma unroll
  for (int u = 0; u < 2; ++u) {
    float sm[4], s1[4], s2[4]; float mx = NEG;
#pragma unroll
    for (int e = 0; e < 4; ++e) {
      sm[e] = q < 2 ? c[0][u][e] * sc : NEG;
      s1[e] = (u == 1 || 4 * q + e <= i) ? c[1][u][e] * sc : NEG;
      s2[e] = (u == 1 && 4 * q + e <= i) ? c[2][u][e] * sc : NEG;
      mx = fmaxf(mx, fmaxf(sm[e], fmaxf(s1[e], s2[e])));
    }
    mx = red32_max(red16_max(mx));
    float sum = 0;
#pragma unroll
    for (int e = 0; e < 4; ++e) {
      sm[e] = q < 2 ? __builtin_amdgcn_exp2f(sm[e] - mx) : 0.f;
      s1[e] = (u == 1 || 4 * q + e <= i) ? __builtin_amdgcn_exp2f(s1[e] - mx) : 0.f;
      s2[e] = (u == 1 && 4 * q + e <= i) ? __builtin_amdgcn_exp2f(s2[e] - mx) : 0.f;
      sum += sm[e] + s1[e] + s2[e];
    }
    sum = red32_add(red16_add(sum));
    const float inv = 1.f / sum;
    pt[u] = u32x4{cvt_pk(s1[0] * inv, s1[1] * inv), cvt_pk(s1[2] * inv, s1[3] * inv), cvt_pk(s2[0] * inv, s2[1] * inv), cvt_pk(s2[2] * inv, s2[3] * inv)};
    pm[u] = u32x4{cvt_pk(sm[0] * inv, sm[1] * inv), cvt_pk(sm[2] * inv, sm[3] * inv), 0u, 0u};
  }
  asm volatile("s_waitcnt lgkmcnt(0)" ::: "memory");
  u16* O = (u16*)(p.ws + WS_O) + (size_t)s * 32 * HD + h * 128;
  const __amdgpu_buffer_rsrc_t ro = __builtin_amdgcn_make_buffer_rsrc((void*)O, 0, 0x7FFFFFFF, 0x00020000);
  const s16x4 zz = {0, 0, 0, 0};
#pragma unroll
  for (int cp = 0; cp < 4; ++cp) {
    f32x4 o[2][2];
#pragma unroll
    for (int c2 = 0; c2 < 2; ++c2) {
      const int ch = 4 * cp + (i & 3);
      const int rm = 4 * q + (i >> 2), r1 = 16 + rm, r2 = 32 + rm;
      const bf16x8 vm = cat4(trread(lds + rm * 256 + 16 * (ch ^ swzb(rm)) + 8 * c2), zz);
      const bf16x8 vt = cat4(trread(lds + r1 * 256 + 16 * (ch ^ swzb(r1)) + 8 * c2), trread(lds + r2 * 256 + 16 * (ch ^ swzb(r2)) + 8 * c2));
#pragma unroll
      for (int u = 0; u < 2; ++u) o[c2][u] = mfma16(vm, asfrag(pm[u]), mfma16(vt, asfrag(pt[u]), f32x4{0, 0, 0, 0}));
    }
#pragma unroll
    for (int u = 0; u < 2; ++u)
      __builtin_amdgcn_raw_buffer_store_b128(u32x4{cvt_pk(o[0][u][0], o[0][u][1]), cvt_pk(o[0][u][2], o[0][u][3]), cvt_pk(o[1][u][0], o[1][u][1]), cvt_pk(o[1][u][2], o[1][u][3])}, ro, ((16 * u + i) * HD + 32 * cp + 8 * q) * 2, 0, 16);
  }
}

template <int BM, int LDA, int LDB, int AUXA>
DEV void small_gemm(const int wv, const u16* A, const u16* B, f32x4 (&acc)[BM / 32][2], char* smem) {
  constexpr int MT = BM / 32, NLD = BM / 64 + 2, NS = (BM == 128) ? 4 : 5;
  const int tid = tid_of(wv);
  const int lane = tid & 63, wr = wv >> 2, wc = wv & 3, fr = lane & 15, fq = lane >> 4;
  const __amdgpu_buffer_rsrc_t rsa = __builtin_amdgcn_make_buffer_rsrc((void*)uni(A), 0, 0x7FFFFFFF, 0x00020000), rsb = __builtin_amdgcn_make_buffer_rsrc((void*)uni(B), 0, 0x7FFFFFFF, 0x00020000);
  u32 voa, vob;
  { int r, c; stage_rc(tid * 16, r, c); voa = (u32)(r * LDA + c) * 2u; const int b = tid * 16, rb = b >> 8; vob = (u32)(rb * LDB + ((((b >> 4) & 15) ^ swzb(rb)) * 8)) * 2u; }
  constexpr int STG = BM * 128 + 16384;
  auto stage = [&](int kt, int slot) {
    char* sa = smem + slot * STG + tid * 16;
    __builtin_amdgcn_raw_ptr_buffer_load_lds(rsa, (LDSP void*)sa, 16, voa, kt * 128, 0, AUXA);
    if (BM == 128) __builtin_amdgcn_raw_ptr_buffer_load_lds(rsa, (LDSP void*)(sa + 8192), 16, voa, kt * 128 + 64 * LDA * 2, 0, AUXA);
    __builtin_amdgcn_raw_ptr_buffer_load_lds(rsb, (LDSP void*)(sa + BM * 128), 16, vob, kt * 64 * LDB * 2, 0, 0);
    __builtin_amdgcn_raw_ptr_buffer_load_lds(rsb, (LDSP void*)(sa + BM * 128 + 8192), 16, vob, (kt * 64 + 32) * LDB * 2, 0, 0);
  };
  int bo[2];
#pragma unroll
  for (int t = 0; t < 2; ++t) { const int row = 8 * fq + 4 * t + (fr >> 2); bo[t] = BM * 128 + 256 * row + 16 * ((4 * wc + (fr & 3)) ^ swzb(row)); }
  const int ao = lds_byte(wr * (BM / 2) + fr, fq * 8);
#pragma unroll
  for (int m = 0; m < MT; ++m) acc[m][0] = acc[m][1] = f32x4{0, 0, 0, 0};
#pragma unroll
  for (int k = 0; k < NS - 1; ++k) stage(k, k);
  constexpr int nt = 16;
  int cur = 0, nxt = NS - 1;
  for (int kt = 0; kt < nt; ++kt) {
    if (kt + NS - 2 < nt) asm volatile("s_waitcnt vmcnt(%0)" ::"n"((NS - 2) * NLD) : "memory"); else asm volatile("s_waitcnt vmcnt(0)" ::: "memory");
    __builtin_amdgcn_s_barrier();
    if (kt + NS - 1 < nt) stage(kt + NS - 1, nxt);
    const char* sa = smem + cur * STG;
#pragma unroll
    for (int k = 0; k < 2; ++k) {
      const bf16x8 b0 = cat4(trread(sa + k * 8192 + bo[0]), trread(sa + k * 8192 + bo[1])), b1 = cat4(trread(sa + k * 8192 + bo[0] + 8), trread(sa + k * 8192 + bo[1] + 8));
#pragma unroll
      for (int m = 0; m < MT; ++m) {
        const bf16x8 a = *(const bf16x8*)(sa + ao + m * 2048 + k * 1024);
        acc[m][0] = mfma16(b0, a, acc[m][0]); acc[m][1] = mfma16(b1, a, acc[m][1]);
      }
    }
    asm volatile("s_waitcnt lgkmcnt(0)" ::: "memory");
    cur = cur == NS - 1 ? 0 : cur + 1; nxt = nxt == NS - 1 ? 0 : nxt + 1;
  }
}
template <int BM>
DEV void wo_item(const int wv, const Params& p, int g, int nt_, int kq, char* smem) {
  constexpr int MT = BM / 32;
  f32x4 acc[MT][2];
  small_gemm<BM, HD, D_, 16>(wv, (const u16*)(p.ws + WS_O) + (size_t)g * BM * HD + kq * 1024, p.Wo + (size_t)kq * 1024 * D_ + nt_ * 128, acc, smem);
  const int tid = tid_of(wv);
  const int lane = tid & 63, wr = wv >> 2, wc = wv & 3, fr = lane & 15, fq = lane >> 4;
  const int tile = (BM == 128 ? g : g - 15) * 8 + nt_;
  char* part = p.ws + WS_P + (size_t)tile * (4 * 65536);
  const __amdgpu_buffer_rsrc_t rp = __builtin_amdgcn_make_buffer_rsrc((void*)part, 0, 0x7FFFFFFF, 0x00020000);
#pragma unroll
  for (int m = 0; m < MT; ++m)
#pragma unroll
    for (int n = 0; n < 2; ++n) __builtin_amdgcn_raw_buffer_store_b128(__builtin_bit_cast(u32x4, acc[m][n]), rp, tid * 16 + (m * 2 + n) * 8192, kq * 65536, 16);
  asm volatile("s_waitcnt vmcnt(0)" ::: "memory");
  __syncthreads();
  volatile int* s_last = (volatile int*)(smem + 128 * 1024 + 32);
  if (tid == 0) *s_last = (int)ctl_add((u32*)(p.ws + WS_CTL) + C_P + tile, 1);
  __syncthreads();
  if (__builtin_amdgcn_readfirstlane(*s_last) != 3) return;
  if (wv == 0) {
    u32* cy = (u32*)(p.ws + WS_CTL) + C_Y + (g * BM >> 8) * 4 + (nt_ >> 1);
    for (int sp = 0; __builtin_amdgcn_readfirstlane(ctl_ld(cy)) < 2; ++sp) { __builtin_amdgcn_s_sleep(6); if (sp > 400000) { ctl_st((u32*)(p.ws + WS_CTL) + C_TMO, 1); break; } }
  }
  __syncthreads();
  const float* Y1 = (const float*)(p.ws + WS_Y); const float* Y2 = (const float*)(p.ws + WS_Y + (8u << 20));
  const __amdgpu_buffer_rsrc_t ry = __builtin_amdgcn_make_buffer_rsrc((void*)Y1, 0, 0x7FFFFFFF, 0x00020000);
#pragma unroll
  for (int m = 0; m < MT; ++m) {
    const u32 o = (u32)(g * BM + wr * (BM / 2) + m * 16 + fr) * D_ + nt_ * 128 + wc * 32 + fq * 8;
    f32x4 v[2];
#pragma unroll
    for (int n = 0; n < 2; ++n) {
      f32x4 t[4];
#pragma unroll
      for (int k = 0; k < 4; ++k) t[k] = __builtin_bit_cast(f32x4, __builtin_amdgcn_raw_buffer_load_b128(rp, tid * 16 + (m * 2 + n) * 8192, k * 65536, 16));
      const f32x4 y1 = __builtin_bit_cast(f32x4, __builtin_amdgcn_raw_buffer_load_b128(ry, o * 4 + n * 16, 0, 16)), y2 = __builtin_bit_cast(f32x4, __builtin_amdgcn_raw_buffer_load_b128(ry, o * 4 + n * 16, 8u << 20, 16));
      v[n] = ((t[0] + t[1]) + (t[2] + t[3])) + (y1 + y2);
    }
    *(u32x4*)(p.out + o) = u32x4{cvt_pk(v[0][0], v[0][1]), cvt_pk(v[0][2], v[0][3]), cvt_pk(v[1][0], v[1][1]), cvt_pk(v[1][2], v[1][3])};
  }
}

DEV void kv0_item(const int wv, const Params& p, int j, char* smem) {
  const int hf = j & 1, mat = (j >> 1) & 1, n0 = (j >> 2) * 128;
  f32x4 acc[4][2];
  small_gemm<128, D_, HD, 0>(wv, (const u16*)(p.ws + WS_XN) + (size_t)hf * 128 * D_, (mat ? p.Wv : p.Wk) + n0, acc, smem);
  const int tid = tid_of(wv), lane = tid & 63, wr = wv >> 2, wc = wv & 3, fr = lane & 15, fq = lane >> 4;
  const __amdgpu_buffer_rsrc_t rc = __builtin_amdgcn_make_buffer_rsrc((void*)((u16*)(p.ws + (mat ? WS_V : WS_K)) + (size_t)hf * 128 * HD + n0), 0, 0x7FFFFFFF, 0x00020000);
#pragma unroll
  for (int m = 0; m < 4; ++m)
    __builtin_amdgcn_raw_buffer_store_b128(u32x4{cvt_pk(acc[m][0][0], acc[m][0][1]), cvt_pk(acc[m][0][2], acc[m][0][3]), cvt_pk(acc[m][1][0], acc[m][1][1]), cvt_pk(acc[m][1][2], acc[m][1][3])},
                                           rc, ((wr * 64 + m * 16 + fr) * HD + wc * 32 + fq * 8) * 2, 0, 16);
}
constexpr int LDS_BYTES = 128 * 1024 + 64;
DEV void wait_cnts(const int wv, u32* ctl, int ia, int na, u32 ta, int ib, int nb, u32 tb) {
  if (wv == 0) {
    const int t = tid_of(0);
    u32* c = ctl + (t < na ? ia + t : (t < na + nb ? ib + t - na : ia));
    const u32 target = t < na ? ta : (t < na + nb ? tb : 0u);
    for (int sp = 0;; ++sp) {
      if (__all(ctl_ld(c) >= target)) break;
      __builtin_amdgcn_s_sleep(6);
      if (sp > 400000 || ((sp & 63) == 63 && __builtin_amdgcn_readfirstlane(ctl_ld(ctl + C_TMO)))) { ctl_st(ctl + C_TMO, 1); break; }
    }
  }
}
DEV void acquire_wg(const int wv) {
  if (tid_of(wv) == 0) { __builtin_amdgcn_fence(__ATOMIC_ACQUIRE, "agent"); asm volatile("s_waitcnt vmcnt(0)" ::: "memory"); }
  __syncthreads();
}
DEV void signal_wg(const int wv, u32* c) {
  asm volatile("s_waitcnt vmcnt(0)" ::: "memory");
  __syncthreads();
  if (tid_of(wv) == 0) ctl_add(c, 1);
}
#define NBIG 0
constexpr int N_KV0 = 128, N_KV = N_KV0 + 224, N_F1 = 112, N_Q = 128, N_Y = 64, N_G = 16 * 48, I_F1 = N_KV, I_Q = I_F1 + N_F1, I_Y = I_Q + N_Q, I_G = I_Y + N_Y, N_ITEMS = I_G + N_G;
typedef __attribute__((address_space(4))) const Params* KArg;
DEV void worker(const int wv, const int role, char* smem) {
  volatile int* s_idx = (volatile int*)(smem + 128 * 1024 + 16);
  int qn = 4, tried = 0;
  for (;;) {
    KArg ka = (KArg)__builtin_amdgcn_kernarg_segment_ptr(); asm volatile("" : "+s"(ka));
    Params p; p.x = ka->x; p.mem0 = ka->mem0; p.Wq = ka->Wq; p.Wk = ka->Wk; p.Wv = ka->Wv; p.Wo = ka->Wo; p.w1 = ka->w1; p.b1 = ka->b1; p.w2 = ka->w2; p.out = ka->out; p.ws = ka->ws;
    u32* ctl = (u32*)(p.ws + WS_CTL);
    const u16* XN = (const u16*)(p.ws + WS_XN);
    __syncthreads();
    if (tid_of(wv) == 0) *s_idx = (int)ctl_add(ctl + C_QUEUE + qn * 32, 1);
    __syncthreads();
    int idx = __builtin_amdgcn_readfirstlane(*s_idx);
    if (qn == 4) { if (idx >= I_G) { qn = (role >> 5) & 3; continue; } }
    else {
      if (idx >= 200) { if (++tried == 4) break; qn = (qn + 1) & 3; continue; }
      int g, k;
      if (idx < 4) { g = 0; k = idx; } else { const int u = idx - 4, gg = u / 12, kk = u - gg * 12;
        if (gg < 15) { if (kk < 4) { g = gg + 1; k = kk; } else { g = gg; k = kk; } } else { g = 15; k = u - 180 + 4; } }
      if (g < 2 * NBIG && k >= 4) {
        if (!(g & 1) || k != 4) continue;
        idx = I_G + N_G + 64 + (g >> 1) * 4 + qn;
      } else
      idx = (g == 15 && k >= 4) ? I_G + N_G + qn * 16 + (k - 4) : I_G + g * 48 + (k < 4 ? k * 4 + qn : 16 + qn * 8 + (k - 4));
    }
    if (idx < N_KV0) {
      kv0_item(wv, p, idx, smem);
      signal_wg(wv, ctl + C_KV);
    } else if (idx < I_G || idx >= I_G + N_G + 64) {
      const u16 *A, *B, *bias; void* C; int lda, ldb, K, ldc, mode; u32* done;
      if (idx < I_F1) { const int jj = idx - N_KV0 + 32, mt = jj >> 5, mat = (jj >> 4) & 1, n0 = (jj & 15) * 256;
        A = XN + (size_t)mt * 256 * D_; lda = D_; B = (mat ? p.Wv : p.Wk) + n0; ldb = HD; K = D_; C = (u16*)(p.ws + (mat ? WS_V : WS_K)) + (size_t)mt * 256 * HD + n0; ldc = HD; bias = p.b1; mode = 0; done = ctl + C_KV + mt * 32;
      } else if (idx < I_Q) { const int j = idx - I_F1, mt = j & 7, n0 = (j >> 3) * 256;
        A = XN + (size_t)mt * 256 * D_; lda = D_; B = p.w1 + n0; ldb = FF; K = D_; C = (u16*)(p.ws + WS_ACT) + (size_t)mt * 256 * FF + n0; ldc = FF; bias = p.b1 + n0; mode = 1; done = ctl + C_F1 + mt * 32;
      } else if (idx < I_Y) { const int j = idx - I_Q, mt = j >> 4, n0 = (j & 15) * 256;
        A = XN + (size_t)mt * 256 * D_; lda = D_; B = p.Wq + n0; ldb = HD; K = D_; C = (u16*)(p.ws + WS_Q) + (size_t)mt * 256 * HD + n0; ldc = HD; bias = p.b1; mode = 0; done = ctl + C_Q + j;
      } else if (idx >= I_G) { const int j = idx - (I_G + N_G + 64), mt = j >> 2, n0 = (j & 3) * 256;
        wait_cnts(wv, ctl, C_A + mt * 8, 8, 4, C_Y + mt * 4 + (j & 3), 1, 2); acquire_wg(wv);
        A = (const u16*)(p.ws + WS_O) + (size_t)mt * 256 * HD; lda = HD; B = p.Wo + n0; ldb = D_; K = HD; C = p.out + (size_t)mt * 256 * D_ + n0; ldc = D_;
        bias = (const u16*)((const float*)(p.ws + WS_Y) + (size_t)mt * 256 * D_ + n0); mode = 4; done = ctl + C_TMO + 1;
      } else { const int j = idx - I_Y, kh = j & 1, mt = j >> 3, n0 = ((j >> 1) & 3) * 256;
        wait_cnts(wv, ctl, C_F1 + mt * 32, 1, 14, 0, 0, 0); acquire_wg(wv);
        A = (const u16*)(p.ws + WS_ACT) + (size_t)mt * 256 * FF + kh * 1792; lda = FF; B = p.w2 + (size_t)kh * 1792 * D_ + n0; ldb = D_; K = 1792;
        C = (float*)(p.ws + WS_Y + (size_t)kh * (8u << 20)) + (size_t)mt * 256 * D_ + n0; ldc = D_; bias = p.x + (size_t)mt * 256 * D_ + n0; mode = kh ? 3 : 2; done = ctl + C_Y + (j >> 1);
      }
      const int dn = __builtin_amdgcn_readfirstlane((int)(done - ctl));
      gemm256(wv, __builtin_amdgcn_readfirstlane(mode), A, __builtin_amdgcn_readfirstlane(lda), B, __builtin_amdgcn_readfirstlane(ldb), __builtin_amdgcn_readfirstlane(K), C, __builtin_amdgcn_readfirstlane(ldc), uni(bias), smem);
      KArg kb = (KArg)__builtin_amdgcn_kernarg_segment_ptr(); asm volatile("" : "+s"(kb));
      signal_wg(wv, (u32*)(kb->ws + WS_CTL) + dn);
    } else if (idx >= I_G + N_G) {
      const int j = idx - I_G - N_G, kq = j >> 4, nt_ = j & 7, hf = (j >> 3) & 1;
      wait_cnts(wv, ctl, C_A + 60 + kq, 1, 4, 0, 0, 0);
      __syncthreads();
      wo_item<64>(wv, p, 30 + hf, nt_, kq, smem);
    } else {
      const int j = idx - I_G, g = j / 48, k = j - g * 48;
      if (k < 16) {
        const int seg = 4 * g + (k >> 2), h0 = 8 * (k & 3);
        wait_cnts(wv, ctl, C_Q + (seg >> 3) * 16 + (h0 >> 1), 4, 1, C_KV + (seg >> 3) * 32, 1, seg < 8 ? 128 : 32);
        __syncthreads();
        attn_item(wv, p, seg, h0 + wv, smem + wv * 12288);
        signal_wg(wv, ctl + C_A + g * 4 + (k & 3));
      } else {
        const int kq = (k - 16) >> 3, nt_ = (k - 16) & 7;
        wait_cnts(wv, ctl, C_A + g * 4 + kq, 1, 4, 0, 0, 0);
        __syncthreads();
        wo_item<128>(wv, p, g, nt_, kq, smem);
      }
    }
  }
}
__global__ void __launch_bounds__(512, 2) k_main(Params p) {
  extern __shared__ __attribute__((aligned(16))) char smem[];
  const int wv = __builtin_amdgcn_readfirstlane(threadIdx.x >> 6);
  const int role = take_role((u32*)(p.ws + WS_CTL), (int*)(smem + 128 * 1024));
  if (role >= 0 && role < 128) chain_role(p, role, smem);
  __syncthreads();
  worker(wv, role, smem);
}
extern "C" void kernel_launch(void* const* d_in, const int* in_sizes, int n_in, void* d_out, int out_size, void* d_ws, size_t ws_size, hipStream_t stream) {
  Params p;
  p.x = (const u16*)d_in[0]; p.mem0 = (const u16*)d_in[1]; p.Wq = (const u16*)d_in[2]; p.Wk = (const u16*)d_in[3]; p.Wv = (const u16*)d_in[4]; p.Wo = (const u16*)d_in[5];
  p.w1 = (const u16*)d_in[8]; p.b1 = (const u16*)d_in[9]; p.w2 = (const u16*)d_in[10]; p.out = (u16*)d_out; p.ws = (char*)d_ws;
  (void)hipFuncSetAttribute((const void*)k_main, hipFuncAttributeMaxDynamicSharedMemorySize, LDS_BYTES);
  k_prep<<<512, 256, 0, stream>>>(p);
  k_main<<<256, 512, LDS_BYTES, stream>>>(p);
}
```
